```python
import torch

def _softmax(x, axis=-1):
    return torch.softmax(x, dim=axis)

def _gelu(x):
    return torch.nn.functional.gelu(x, approximate='tanh')

def _silu(x):
    return torch.nn.functional.silu(x)

def _sigmoid(x):
    return torch.sigmoid(x)

def _layernorm(x, axis=-1, eps=1e-05):
    m = x.mean(dim=axis, keepdim=True)
    v = x.var(dim=axis, keepdim=True, unbiased=False)
    return (x - m) / torch.sqrt(v + eps)

def _scatter_add(tgt, idx, src, axis=0):
    return tgt.clone().index_add_(axis, idx, src)

def _segment_sum(src, seg, n):
    out = torch.zeros((n,) + src.shape[1:], dtype=src.dtype)
    return out.index_add_(0, seg, src)

def _pad(x, pw):
    flat = tuple((v for pair in reversed(pw) for v in pair))
    return torch.nn.functional.pad(x, flat)
kernel_name = 'adaLN_cond__memory_aug_recurrent_mem_tr__mixer_gmlp__prompt'

def setup_inputs():

    _w = lambda a: (lambda t: t.to(torch.bfloat16) if t.is_floating_point() else t)(torch.as_tensor(a))

    def _A_setup():
        B, S, Dm = (1, 2048, 512)
        H, Dh = (32, 128)
        M, seg = (8, 32)
        seg = min(seg, S)
        S = S // seg * seg
        return {'x': _w((lambda *s: torch.randn(*s, dtype=torch.bfloat16) * 0.05)(B, S, Dm)), 'mem0': _w((lambda *s: torch.randn(*s, dtype=torch.bfloat16) * 0.4)(B, M, Dm)), 'Wq': _w((lambda *s: torch.randn(*s, dtype=torch.bfloat16) * 3.2)(Dm, H, Dh) * Dm ** (-0.5)), 'Wk': _w((lambda *s: torch.randn(*s, dtype=torch.bfloat16) * 3.2)(Dm, H, Dh) * Dm ** (-0.5)), 'Wv': _w((lambda *s: torch.randn(*s, dtype=torch.bfloat16) * 0.4)(Dm, H, Dh) * Dm ** (-0.5)), 'Wo': _w((lambda *s: torch.randn(*s, dtype=torch.bfloat16) * 0.4)(H, Dh, Dm) * (H * Dh) ** (-0.5)), 'seg_len': seg}
    _A_in = _A_setup()

    def _B_setup():
        B, S, D, F = (1, 2048, 512, 1376)
        Fh = F // 2
        return {'x': _w((lambda *s: torch.randn(*s, dtype=torch.bfloat16) * 0.05)(B, S, D)), 'w_in': _w((lambda *s: torch.randn(*s, dtype=torch.bfloat16) * 0.8)(D, 2 * Fh)), 'w_sp': _w(0.01 * (lambda *s: torch.randn(*s, dtype=torch.bfloat16) * 0.8)(S, S)), 'b_sp': _w(1.0 + 0.01 * (lambda *s: torch.randn(*s, dtype=torch.bfloat16) * 0.05)(S)), 'w_out': _w((lambda *s: torch.randn(*s, dtype=torch.bfloat16) * 0.05)(Fh, D))}
    _B_in = _B_setup()
    return {'A_x': _A_in['x'], 'A_mem0': _A_in['mem0'], 'A_Wq': _A_in['Wq'], 'A_Wk': _A_in['Wk'], 'A_Wv': _A_in['Wv'], 'A_Wo': _A_in['Wo'], 'A_seg_len': _A_in['seg_len'], 'B_x': _B_in['x'], 'B_w_in': _B_in['w_in'], 'B_w_sp': _B_in['w_sp'], 'B_b_sp': _B_in['b_sp'], 'B_w_out': _B_in['w_out'], 'c': _w((lambda *s: torch.randn(*s, dtype=torch.bfloat16) * 0.1)(1, 512) * 8.0), 'w_mod': _w((lambda *s: torch.randn(*s, dtype=torch.bfloat16) * 0.1)(512, 6 * 512) * 512 ** (-0.5) * 8.0)}

def reference(A_x, A_mem0, A_Wq, A_Wk, A_Wv, A_Wo, A_seg_len, B_x, B_w_in, B_w_sp, B_b_sp, B_w_out, c, w_mod):

    def _A_ref(x, mem0, Wq, Wk, Wv, Wo, seg_len):
        B, S, Dm = x.shape
        H, Dh = (Wq.shape[1], Wq.shape[2])
        M = mem0.shape[1]
        scale = Dh ** (-0.5)
        n_seg = S // seg_len
        L = M + seg_len
        idx = torch.arange(L)
        mask = (idx[:, None] < M) | (idx[:, None] >= idx[None, :])
        mem = mem0
        outs = []
        for s in range(n_seg):
            xs = x[:, s * seg_len:(s + 1) * seg_len, :].clone()
            h = torch.concatenate([mem, xs], axis=1)
            q = torch.einsum('btd,dhe->bhte', h, Wq)
            k = torch.einsum('btd,dhe->bhte', h, Wk)
            v = torch.einsum('btd,dhe->bhte', h, Wv)
            scores = torch.einsum('bhqe,bhke->bhqk', q, k) * scale
            scores = torch.where(mask[None, None], scores, -1000000000.0)
            p = _softmax(scores, axis=-1)
            o = torch.einsum('bhqk,bhke->bhqe', p, v)
            o = torch.einsum('bhte,hed->btd', o, Wo)
            mem = o[:, :M, :].clone()
            outs.append(o[:, M:, :])
        return torch.concatenate(outs, axis=1)

    def _B_ref(x, w_in, w_sp, b_sp, w_out):
        B, S, D = x.shape
        F = w_in.shape[-1]
        Fh = F // 2
        z = _gelu(torch.einsum('bsd,df->bsf', x, w_in))
        z1 = z[..., :Fh].clone()
        z2 = _layernorm(z[..., Fh:], axis=-1)
        g = torch.einsum('ts,bsf->btf', w_sp, z2) + b_sp[None, :, None]
        return torch.einsum('bsf,fd->bsd', z1 * g, w_out)
    x = A_x
    mod = (_silu(c) @ w_mod).reshape(c.shape[0], 6, 1, -1)
    sa, ca, ga, sb, cb, gb = (mod[:, 0].clone(), mod[:, 1].clone(), mod[:, 2].clone(), mod[:, 3].clone(), mod[:, 4].clone(), mod[:, 5].clone())
    h = _layernorm(x, axis=-1) * (1 + ca) + sa
    x = x + ga * _A_ref(h, A_mem0, A_Wq, A_Wk, A_Wv, A_Wo, A_seg_len)
    h = _layernorm(x, axis=-1) * (1 + cb) + sb
    x = x + gb * _B_ref(h, B_w_in, B_w_sp, B_b_sp, B_w_out)
    return x
```

```cpp
#include <hip/hip_runtime.h>
#include <hip/hip_bf16.h>
#include <stdint.h>

typedef __bf16 bf16;
typedef __bf16 bf16x8 __attribute__((ext_vector_type(8)));
typedef __bf16 bf16x4 __attribute__((ext_vector_type(4)));
typedef float f32x4 __attribute__((ext_vector_type(4)));
typedef float f32x16 __attribute__((ext_vector_type(16)));
typedef unsigned u32x4 __attribute__((ext_vector_type(4)));
typedef short s16x4 __attribute__((ext_vector_type(4)));

constexpr int S_ = 2048, D_ = 512, H_ = 32, DH = 128, M_ = 8, SEG = 32, NSEG = 64, HD = 4096, F2 = 1376, FH = 688;

__device__ inline float wave_sum(float v){ for(int o=32;o>0;o>>=1) v += __shfl_xor(v,o); return v; }

__global__ __launch_bounds__(256) void k_mod(const bf16* c, const bf16* w, float* mod){
  __shared__ float red[32][65];
  int t=threadIdx.x, cg=t&7, kg=t>>3; int n0=blockIdx.x*64+cg*8;
  float acc[8]={0,0,0,0,0,0,0,0};
  for(int i=0;i<16;i++){ int k=kg*16+i; float cv=(float)c[k]; float s=(float)(bf16)(cv/(1.f+__expf(-cv)));
    bf16x8 wv=*(const bf16x8*)(w+(size_t)k*3072+n0);
    for(int j=0;j<8;j++) acc[j]+=s*(float)wv[j]; }
  for(int j=0;j<8;j++) red[kg][cg*8+j]=acc[j];
  __syncthreads();
  if(t<64){ float s=0; for(int i=0;i<32;i++) s+=red[i][t]; mod[blockIdx.x*64+t]=(float)(bf16)s; }
}
template<bool F32IN> __global__ __launch_bounds__(256) void k_ln(const void* xin, const float* sh, const float* sc, bf16* out){
  int row=blockIdx.x*4+(threadIdx.x>>6), l=threadIdx.x&63; float v[8];
  if(F32IN){ const float* p=(const float*)xin+(size_t)row*512+l*8; for(int j=0;j<8;j++) v[j]=p[j]; }
  else { bf16x8 x=*(const bf16x8*)((const bf16*)xin+(size_t)row*512+l*8); for(int j=0;j<8;j++) v[j]=(float)x[j]; }
  float s=0; for(int j=0;j<8;j++) s+=v[j]; float m=wave_sum(s)*(1.f/512);
  float q=0; for(int j=0;j<8;j++){ float d=v[j]-m; q+=d*d; } float r=rsqrtf(wave_sum(q)*(1.f/512)+1e-5f);
  bf16x8 o; for(int j=0;j<8;j++) o[j]=(bf16)((v[j]-m)*r*(1.f+sc[l*8+j])+sh[l*8+j]);
  *(bf16x8*)(out+(size_t)row*512+l*8)=o;
}
__global__ __launch_bounds__(256) void k_lnz(const bf16* z, bf16* out){
  int row=blockIdx.x*4+(threadIdx.x>>6), l=threadIdx.x&63; float v[11]; float s=0;
  for(int j=0;j<11;j++){ int c=l+64*j; v[j]= c<FH? (float)z[(size_t)row*F2+FH+c]:0.f; s+=v[j]; }
  float m=wave_sum(s)*(1.f/FH); float q=0;
  for(int j=0;j<11;j++){ int c=l+64*j; float d= c<FH? v[j]-m:0.f; q+=d*d; }
  float r=rsqrtf(wave_sum(q)*(1.f/FH)+1e-5f);
  for(int j=0;j<11;j++){ int c=l+64*j; if(c<FH) out[(size_t)row*FH+c]=(bf16)((v[j]-m)*r); }
}
template<int EPI> __global__ __launch_bounds__(256) void k_gemm(const bf16* A, int lda, const bf16* B, int ldb, int N, int K, void* C, int ldc,
     const void* e0, const void* e1){
  __shared__ bf16 As[64][40]; __shared__ bf16 Bs[64][40];
  int t=threadIdx.x, w=t>>6, l=t&63; int m0=blockIdx.y*64, n0=blockIdx.x*64;
  int wm=(w>>1)*32, wn=(w&1)*32;
  f32x16 acc; for(int i=0;i<16;i++) acc[i]=0;
  for(int k0=0;k0<K;k0+=32){
    { int r=t>>2, c=(t&3)*8; bf16x8 v; if(k0+c<K) v=*(const bf16x8*)(A+(size_t)(m0+r)*lda+k0+c); else for(int j=0;j<8;j++) v[j]=(bf16)0.f;
      *(bf16x8*)&As[r][c]=v; }
    { int k=t>>3, c=(t&7)*8; bf16x8 v; if(k0+k<K && n0+c<N) v=*(const bf16x8*)(B+(size_t)(k0+k)*ldb+n0+c); else for(int j=0;j<8;j++) v[j]=(bf16)0.f;
      for(int j=0;j<8;j++) Bs[c+j][k]=v[j]; }
    __syncthreads();
    for(int kk=0;kk<32;kk+=16){
      bf16x8 a=*(bf16x8*)&As[wm+(l&31)][kk+8*(l>>5)];
      bf16x8 b=*(bf16x8*)&Bs[wn+(l&31)][kk+8*(l>>5)];
      acc=__builtin_amdgcn_mfma_f32_32x32x16_bf16(a,b,acc,0,0,0);
    }
    __syncthreads();
  }
  int col=n0+wn+(l&31);
  if(col<N) for(int i=0;i<16;i++){ int row=m0+wm+(i&3)+8*(i>>2)+4*(l>>5); float v=acc[i]; size_t o=(size_t)row*ldc+col;
    if(EPI==0) ((bf16*)C)[o]=(bf16)v;
    if(EPI==1) ((float*)C)[o]=(float)((const bf16*)e0)[o]+((const float*)e1)[col]*v;
    if(EPI==2){ float u=0.7978845608f*(v+0.044715f*v*v*v); float th=1.f-2.f/(1.f+__expf(2.f*u)); ((bf16*)C)[o]=(bf16)(0.5f*v*(1.f+th)); }
    if(EPI==3) ((bf16*)C)[o]=(bf16)((float)((const bf16*)e0)[(size_t)row*F2+col]*(v+(float)((const bf16*)e1)[row]));
    if(EPI==4) ((bf16*)C)[o]=(bf16)(((const float*)e0)[o]+((const float*)e1)[col]*v);
  }
}
__global__ __launch_bounds__(256) void k_step(int s, const bf16* mem0, const float* pin, float* pout, const bf16* Wq, const bf16* Wk, const bf16* Wv, const bf16* Wo,
     const bf16* Kx, const bf16* Vx, bf16* km, bf16* vm){
  __shared__ float mem[8][512]; __shared__ float qkv[3][8][128]; __shared__ float sc[8][40]; __shared__ float o[8][128];
  int h=blockIdx.x, t=threadIdx.x;
  for(int i=t;i<4096;i+=256){ float v; if(s==0) v=(float)mem0[i]; else { v=0; for(int p=0;p<32;p++) v+=pin[p*4096+i]; v=(float)(bf16)v; } mem[i>>9][i&511]=v; }
  __syncthreads();
  for(int j=t;j<384;j+=256){ int m=j>>7, e=j&127; const bf16* W=(m==0?Wq:(m==1?Wk:Wv))+h*128+e; float a[8]={0,0,0,0,0,0,0,0};
    for(int d=0;d<512;d++){ float wv=(float)W[(size_t)d*HD]; for(int r=0;r<8;r++) a[r]+=mem[r][d]*wv; }
    for(int r=0;r<8;r++){ bf16 b=(bf16)a[r]; qkv[m][r][e]=(float)b; if(m==1) km[((size_t)s*8+r)*HD+h*128+e]=b; if(m==2) vm[((size_t)s*8+r)*HD+h*128+e]=b; } }
  __syncthreads();
  for(int j=t;j<320;j+=256){ int r=j/40, k=j%40; float a=0;
    if(k<8) for(int e=0;e<128;e++) a+=qkv[0][r][e]*qkv[1][k][e];
    else { const bf16* kp=Kx+(size_t)(s*32+k-8)*HD+h*128; for(int e=0;e<128;e++) a+=qkv[0][r][e]*(float)kp[e]; }
    sc[r][k]=a*0.08838834764f; }
  __syncthreads();
  if(t<8){ float mx=-1e30f; for(int k=0;k<40;k++) mx=fmaxf(mx,sc[t][k]); float su=0; for(int k=0;k<40;k++){ float e=__expf(sc[t][k]-mx); sc[t][k]=e; su+=e; }
    for(int k=0;k<40;k++) sc[t][k]=(float)(bf16)(sc[t][k]/su); }
  __syncthreads();
  for(int j=t;j<1024;j+=256){ int r=j>>7,e=j&127; float a=0; for(int k=0;k<8;k++) a+=sc[r][k]*qkv[2][k][e];
    for(int k=0;k<32;k++) a+=sc[r][8+k]*(float)Vx[(size_t)(s*32+k)*HD+h*128+e]; o[r][e]=(float)(bf16)a; }
  __syncthreads();
  for(int n=t;n<512;n+=256){ float a[8]={0,0,0,0,0,0,0,0}; for(int e=0;e<128;e++){ float wv=(float)Wo[(size_t)(h*128+e)*512+n]; for(int r=0;r<8;r++) a[r]+=o[r][e]*wv; }
    for(int r=0;r<8;r++) pout[h*4096+r*512+n]=a[r]; }
}
__global__ __launch_bounds__(256) void k_attn(const bf16* Q, const bf16* Kx, const bf16* Vx, const bf16* km, const bf16* vm, bf16* O){
  __shared__ float q[32][129], k[40][129], v[40][128], sc[32][41];
  int s=blockIdx.x>>5, h=blockIdx.x&31, t=threadIdx.x;
  for(int i=t;i<4096;i+=256){ int r=i>>7,e=i&127; size_t g=(size_t)(s*32+r)*HD+h*128+e; q[r][e]=(float)Q[g]; k[8+r][e]=(float)Kx[g]; v[8+r][e]=(float)Vx[g]; }
  for(int i=t;i<1024;i+=256){ int r=i>>7,e=i&127; size_t g=((size_t)s*8+r)*HD+h*128+e; k[r][e]=(float)km[g]; v[r][e]=(float)vm[g]; }
  __syncthreads();
  for(int j=t;j<1280;j+=256){ int r=j/40,c=j%40; float a=0; for(int e=0;e<128;e++) a+=q[r][e]*k[c][e]; sc[r][c]= (c<8 || c-8<=r)? a*0.08838834764f : -1e9f; }
  __syncthreads();
  if(t<32){ float mx=-1e30f; for(int c=0;c<40;c++) mx=fmaxf(mx,sc[t][c]); float su=0; for(int c=0;c<40;c++){ float e=__expf(sc[t][c]-mx); sc[t][c]=e; su+=e; }
    for(int c=0;c<40;c++) sc[t][c]=(float)(bf16)(sc[t][c]/su); }
  __syncthreads();
  for(int j=t;j<4096;j+=256){ int r=j>>7,e=j&127; float a=0; for(int c=0;c<40;c++) a+=sc[r][c]*v[c][e]; O[(size_t)(s*32+r)*HD+h*128+e]=(bf16)a; }
}

extern "C" void kernel_launch(void* const* d_in, const int* in_sizes, int n_in, void* d_out, int out_size, void* d_ws, size_t ws_size, hipStream_t stream){
  const bf16* x=(const bf16*)d_in[0]; const bf16* mem0=(const bf16*)d_in[1];
  const bf16* Wq=(const bf16*)d_in[2]; const bf16* Wk=(const bf16*)d_in[3]; const bf16* Wv=(const bf16*)d_in[4]; const bf16* Wo=(const bf16*)d_in[5];
  const bf16* w_in=(const bf16*)d_in[8]; const bf16* w_sp=(const bf16*)d_in[9]; const bf16* b_sp=(const bf16*)d_in[10]; const bf16* w_out=(const bf16*)d_in[11];
  const bf16* c=(const bf16*)d_in[12]; const bf16* w_mod=(const bf16*)d_in[13];
  char* ws=(char*)d_ws; size_t MB=1<<20;
  float* mod=(float*)ws; bf16* h=(bf16*)(ws+1*MB); bf16* Q=(bf16*)(ws+4*MB); bf16* Kx=(bf16*)(ws+20*MB); bf16* Vx=(bf16*)(ws+36*MB); bf16* O=(bf16*)(ws+52*MB);
  bf16* km=(bf16*)(ws+68*MB); bf16* vm=(bf16*)(ws+72*MB); float* part=(float*)(ws+76*MB); float* x1=(float*)(ws+78*MB); bf16* h2=(bf16*)(ws+82*MB);
  bf16* z=(bf16*)(ws+84*MB); bf16* z2n=(bf16*)(ws+90*MB); bf16* zg=(bf16*)(ws+94*MB);
  k_mod<<<48,256,0,stream>>>(c,w_mod,mod);
  k_ln<false><<<512,256,0,stream>>>(x,mod,mod+512,h);
  k_gemm<0><<<dim3(64,32),256,0,stream>>>(h,512,Wq,HD,HD,512,Q,HD,nullptr,nullptr);
  k_gemm<0><<<dim3(64,32),256,0,stream>>>(h,512,Wk,HD,HD,512,Kx,HD,nullptr,nullptr);
  k_gemm<0><<<dim3(64,32),256,0,stream>>>(h,512,Wv,HD,HD,512,Vx,HD,nullptr,nullptr);
  for(int s=0;s<64;s++) k_step<<<32,256,0,stream>>>(s,mem0,part+((s+1)&1)*32*4096,part+(s&1)*32*4096,Wq,Wk,Wv,Wo,Kx,Vx,km,vm);
  k_attn<<<2048,256,0,stream>>>(Q,Kx,Vx,km,vm,O);
  k_gemm<1><<<dim3(8,32),256,0,stream>>>(O,HD,Wo,512,512,HD,x1,512,x,mod+1024);
  k_ln<true><<<512,256,0,stream>>>(x1,mod+1536,mod+2048,h2);
  k_gemm<2><<<dim3(22,32),256,0,stream>>>(h2,512,w_in,F2,F2,512,z,F2,nullptr,nullptr);
  k_lnz<<<512,256,0,stream>>>(z,z2n);
  k_gemm<3><<<dim3(11,32),256,0,stream>>>(w_sp,2048,z2n,FH,FH,2048,zg,FH,z,b_sp);
  k_gemm<4><<<dim3(8,32),256,0,stream>>>(zg,FH,w_out,512,512,FH,d_out,512,x1,mod+2560);
}
```

```cpp
#include <hip/hip_runtime.h>
#include <stdint.h>
#include <type_traits>
#define SPR
#define NPR 4
#define CHAIN_ASM \
"v_mbcnt_lo_u32_b32 v8, -1, 0\n" \
"v_mbcnt_hi_u32_b32 v8, -1, v8\n" \
"s_lshl_b32 s70, %0, 10\n" \
"s_lshl_b32 s71, %0, 15\n" \
"s_add_u32 s71, s71, 32768\n" \
"v_lshlrev_b32 v9, 2, v8\n" \
"v_lshlrev_b32 v8, 4, v8\n" \
"v_add_u32 v8, s70, v8\n" \
"v_add_u32 v9, s71, v9\n" \
"ds_read_b32 v9, v9\n" \
"ds_read_b128 v[0:3], v8 offset:0\n" \
"ds_read_b128 v[4:7], v8 offset:4096\n" \
"ds_read_b128 v[108:111], v8 offset:8192\n" \
"ds_read_b128 v[112:115], v8 offset:12288\n" \
"ds_read_b128 v[116:119], v8 offset:16384\n" \
"ds_read_b128 v[120:123], v8 offset:20480\n" \
"ds_read_b128 v[124:127], v8 offset:24576\n" \
"ds_read_b128 v[32:35], v8 offset:28672\n" \
"v_mov_b32 v104, 0x3f803f80\n" \
"v_mov_b32 v105, 0x3f803f80\n" \
"v_mov_b32 v106, 0x3f803f80\n" \
"v_mov_b32 v107, 0x3f803f80\n" \
"s_waitcnt lgkmcnt(0)\n" \
"v_readlane_b32 s40, v9, 0\n" \
"v_readlane_b32 s41, v9, 1\n" \
"v_readlane_b32 s42, v9, 2\n" \
"v_readlane_b32 s43, v9, 3\n" \
"v_readlane_b32 s44, v9, 4\n" \
"v_readlane_b32 s45, v9, 5\n" \
"v_readlane_b32 s46, v9, 6\n" \
"v_readlane_b32 s47, v9, 7\n" \
"v_readlane_b32 s48, v9, 8\n" \
"v_readlane_b32 s49, v9, 9\n" \
"v_readlane_b32 s50, v9, 10\n" \
"v_readlane_b32 s51, v9, 11\n" \
"v_readlane_b32 s52, v9, 12\n" \
"v_readlane_b32 s53, v9, 13\n" \
"v_readlane_b32 s54, v9, 14\n" \
"v_readlane_b32 s55, v9, 15\n" \
"v_readlane_b32 s56, v9, 16\n" \
"v_readlane_b32 s57, v9, 17\n" \
"v_readlane_b32 s58, v9, 18\n" \
"v_readlane_b32 s59, v9, 19\n" \
"v_readlane_b32 s60, v9, 20\n" \
"v_readlane_b32 s61, v9, 21\n" \
"v_readlane_b32 s62, v9, 22\n" \
"v_readlane_b32 s63, v9, 23\n" \
"v_readlane_b32 s64, v9, 24\n" \
"v_readlane_b32 s65, v9, 25\n" \
"v_readlane_b32 s66, v9, 26\n" \
"v_readlane_b32 s67, v9, 27\n" \
"v_readlane_b32 s68, v9, 28\n" \
"v_readlane_b32 s69, v9, 29\n" \
"v_readlane_b32 s36, v9, 30\n" \
"v_readlane_b32 s37, v9, 31\n" \
"v_readlane_b32 s38, v9, 32\n" \
"v_readlane_b32 s39, v9, 33\n" \
"v_add_u32 v36, 0, v32\n" \
"v_add_u32 v40, 0, v33\n" \
"v_add_u32 v37, 15360, v32\n" \
"v_add_u32 v41, 15360, v33\n" \
"v_add_u32 v38, 63488, v32\n" \
"v_add_u32 v42, 30720, v33\n" \
"v_add_u32 v39, 78848, v32\n" \
"v_add_u32 v43, 46080, v33\n" \
"s_xor_b32 s70, s62, 1\n" \
"s_lshl_b32 s70, s70, 20\n" \
"s_add_u32 s70, s70, 0\n" \
"s_add_u32 s72, s40, s70\n" \
"s_addc_u32 s73, s41, 0\n" \
"s_add_u32 m0, s71, 0\n" \
"s_nop 0\n" \
"global_load_lds_dwordx4 v36, s[72:73] offset:0\n" \
"global_load_lds_dwordx4 v37, s[72:73] offset:1024\n" \
"global_load_lds_dwordx4 v38, s[72:73] offset:2048\n" \
"global_load_lds_dwordx4 v39, s[72:73] offset:3072\n" \
"s_xor_b32 s70, s62, 1\n" \
"s_lshl_b32 s70, s70, 20\n" \
"s_add_u32 s70, s70, 524288\n" \
"s_add_u32 s72, s40, s70\n" \
"s_addc_u32 s73, s41, 0\n" \
"s_add_u32 m0, s71, 4096\n" \
"s_nop 0\n" \
"global_load_lds_dwordx4 v36, s[72:73] offset:0\n" \
"global_load_lds_dwordx4 v37, s[72:73] offset:1024\n" \
"global_load_lds_dwordx4 v38, s[72:73] offset:2048\n" \
"global_load_lds_dwordx4 v39, s[72:73] offset:3072\n" \
"s_xor_b32 s70, s62, 2\n" \
"s_lshl_b32 s70, s70, 20\n" \
"s_add_u32 s70, s70, 0\n" \
"s_add_u32 s72, s40, s70\n" \
"s_addc_u32 s73, s41, 0\n" \
"s_add_u32 m0, s71, 8192\n" \
"s_nop 0\n" \
"global_load_lds_dwordx4 v36, s[72:73] offset:0\n" \
"global_load_lds_dwordx4 v37, s[72:73] offset:1024\n" \
"global_load_lds_dwordx4 v38, s[72:73] offset:2048\n" \
"global_load_lds_dwordx4 v39, s[72:73] offset:3072\n" \
"s_xor_b32 s70, s62, 2\n" \
"s_lshl_b32 s70, s70, 20\n" \
"s_add_u32 s70, s70, 524288\n" \
"s_add_u32 s72, s40, s70\n" \
"s_addc_u32 s73, s41, 0\n" \
"s_add_u32 m0, s71, 12288\n" \
"s_nop 0\n" \
"global_load_lds_dwordx4 v36, s[72:73] offset:0\n" \
"global_load_lds_dwordx4 v37, s[72:73] offset:1024\n" \
"global_load_lds_dwordx4 v38, s[72:73] offset:2048\n" \
"global_load_lds_dwordx4 v39, s[72:73] offset:3072\n" \
"s_xor_b32 s70, s62, 3\n" \
"s_lshl_b32 s70, s70, 20\n" \
"s_add_u32 s70, s70, 0\n" \
"s_add_u32 s72, s40, s70\n" \
"s_addc_u32 s73, s41, 0\n" \
"s_add_u32 m0, s71, 16384\n" \
"s_nop 0\n" \
"global_load_lds_dwordx4 v36, s[72:73] offset:0\n" \
"global_load_lds_dwordx4 v37, s[72:73] offset:1024\n" \
"global_load_lds_dwordx4 v38, s[72:73] offset:2048\n" \
"global_load_lds_dwordx4 v39, s[72:73] offset:3072\n" \
"s_xor_b32 s70, s62, 0\n" \
"s_lshl_b32 s70, s70, 20\n" \
"s_add_u32 s70, s70, 524288\n" \
"s_add_u32 s72, s42, s70\n" \
"s_addc_u32 s73, s43, 0\n" \
"s_add_u32 m0, s71, 20480\n" \
"s_nop 0\n" \
"global_load_lds_dwordx4 v36, s[72:73] offset:0\n" \
"global_load_lds_dwordx4 v37, s[72:73] offset:1024\n" \
"global_load_lds_dwordx4 v38, s[72:73] offset:2048\n" \
"global_load_lds_dwordx4 v39, s[72:73] offset:3072\n" \
"s_xor_b32 s70, s62, 1\n" \
"s_lshl_b32 s70, s70, 20\n" \
"s_add_u32 s70, s70, 0\n" \
"s_add_u32 s72, s42, s70\n" \
"s_addc_u32 s73, s43, 0\n" \
"s_add_u32 m0, s71, 24576\n" \
"s_nop 0\n" \
"global_load_lds_dwordx4 v36, s[72:73] offset:0\n" \
"global_load_lds_dwordx4 v37, s[72:73] offset:1024\n" \
"global_load_lds_dwordx4 v38, s[72:73] offset:2048\n" \
"global_load_lds_dwordx4 v39, s[72:73] offset:3072\n" \
"s_xor_b32 s70, s62, 1\n" \
"s_lshl_b32 s70, s70, 20\n" \
"s_add_u32 s70, s70, 524288\n" \
"s_add_u32 s72, s42, s70\n" \
"s_addc_u32 s73, s43, 0\n" \
"s_add_u32 m0, s71, 28672\n" \
"s_nop 0\n" \
"global_load_lds_dwordx4 v36, s[72:73] offset:0\n" \
"global_load_lds_dwordx4 v37, s[72:73] offset:1024\n" \
"global_load_lds_dwordx4 v38, s[72:73] offset:2048\n" \
"global_load_lds_dwordx4 v39, s[72:73] offset:3072\n" \
"s_waitcnt vmcnt(32)\n" \
"s_barrier\n" \
"s_cmp_lg_u32 s62, 0\n" \
"s_cbranch_scc1 Lnoflag%=\n" \
"v_mov_b32 v44, s66\n" \
"v_mov_b32 v45, s63\n" \
"s_mov_b64 exec, 1\n" \
"global_store_dword v44, v45, s[58:59] sc1\n" \
"s_mov_b64 exec, -1\n" \
"Lnoflag%=:\n" \
"s_waitcnt vmcnt(28)\n" \
"ds_read_b64_tr_b16 a[0:1], v34 offset:0\n" \
"ds_read_b64_tr_b16 a[2:3], v34 offset:256\n" \
"ds_read_b64_tr_b16 a[4:5], v34 offset:2048\n" \
"ds_read_b64_tr_b16 a[6:7], v34 offset:2304\n" \
"ds_read_b64_tr_b16 a[8:9], v34 offset:32\n" \
"ds_read_b64_tr_b16 a[10:11], v34 offset:288\n" \
"ds_read_b64_tr_b16 a[12:13], v34 offset:2080\n" \
"ds_read_b64_tr_b16 a[14:15], v34 offset:2336\n" \
"s_waitcnt lgkmcnt(0)\n" \
"s_xor_b32 s70, s62, 2\n" \
"s_lshl_b32 s70, s70, 20\n" \
"s_add_u32 s70, s70, 0\n" \
"s_add_u32 s72, s42, s70\n" \
"s_addc_u32 s73, s43, 0\n" \
"s_add_u32 m0, s71, 0\n" \
"s_nop 0\n" \
"global_load_lds_dwordx4 v36, s[72:73] offset:0\n" \
"global_load_lds_dwordx4 v37, s[72:73] offset:1024\n" \
"global_load_lds_dwordx4 v38, s[72:73] offset:2048\n" \
"global_load_lds_dwordx4 v39, s[72:73] offset:3072\n" \
"s_waitcnt vmcnt(28)\n" \
"ds_read_b64_tr_b16 a[16:17], v34 offset:4096\n" \
"ds_read_b64_tr_b16 a[18:19], v34 offset:4352\n" \
"ds_read_b64_tr_b16 a[20:21], v34 offset:6144\n" \
"ds_read_b64_tr_b16 a[22:23], v34 offset:6400\n" \
"ds_read_b64_tr_b16 a[24:25], v34 offset:4128\n" \
"ds_read_b64_tr_b16 a[26:27], v34 offset:4384\n" \
"ds_read_b64_tr_b16 a[28:29], v34 offset:6176\n" \
"ds_read_b64_tr_b16 a[30:31], v34 offset:6432\n" \
"s_waitcnt lgkmcnt(0)\n" \
"s_xor_b32 s70, s62, 2\n" \
"s_lshl_b32 s70, s70, 20\n" \
"s_add_u32 s70, s70, 524288\n" \
"s_add_u32 s72, s42, s70\n" \
"s_addc_u32 s73, s43, 0\n" \
"s_add_u32 m0, s71, 4096\n" \
"s_nop 0\n" \
"global_load_lds_dwordx4 v36, s[72:73] offset:0\n" \
"global_load_lds_dwordx4 v37, s[72:73] offset:1024\n" \
"global_load_lds_dwordx4 v38, s[72:73] offset:2048\n" \
"global_load_lds_dwordx4 v39, s[72:73] offset:3072\n" \
"s_waitcnt vmcnt(28)\n" \
"ds_read_b64_tr_b16 a[32:33], v34 offset:8192\n" \
"ds_read_b64_tr_b16 a[34:35], v34 offset:8448\n" \
"ds_read_b64_tr_b16 a[36:37], v34 offset:10240\n" \
"ds_read_b64_tr_b16 a[38:39], v34 offset:10496\n" \
"ds_read_b64_tr_b16 a[40:41], v34 offset:8224\n" \
"ds_read_b64_tr_b16 a[42:43], v34 offset:8480\n" \
"ds_read_b64_tr_b16 a[44:45], v34 offset:10272\n" \
"ds_read_b64_tr_b16 a[46:47], v34 offset:10528\n" \
"s_waitcnt lgkmcnt(0)\n" \
"s_xor_b32 s70, s62, 3\n" \
"s_lshl_b32 s70, s70, 20\n" \
"s_add_u32 s70, s70, 0\n" \
"s_add_u32 s72, s42, s70\n" \
"s_addc_u32 s73, s43, 0\n" \
"s_add_u32 m0, s71, 8192\n" \
"s_nop 0\n" \
"global_load_lds_dwordx4 v36, s[72:73] offset:0\n" \
"global_load_lds_dwordx4 v37, s[72:73] offset:1024\n" \
"global_load_lds_dwordx4 v38, s[72:73] offset:2048\n" \
"global_load_lds_dwordx4 v39, s[72:73] offset:3072\n" \
"s_waitcnt vmcnt(28)\n" \
"ds_read_b64_tr_b16 a[48:49], v34 offset:12288\n" \
"ds_read_b64_tr_b16 a[50:51], v34 offset:12544\n" \
"ds_read_b64_tr_b16 a[52:53], v34 offset:14336\n" \
"ds_read_b64_tr_b16 a[54:55], v34 offset:14592\n" \
"ds_read_b64_tr_b16 a[56:57], v34 offset:12320\n" \
"ds_read_b64_tr_b16 a[58:59], v34 offset:12576\n" \
"ds_read_b64_tr_b16 a[60:61], v34 offset:14368\n" \
"ds_read_b64_tr_b16 a[62:63], v34 offset:14624\n" \
"s_waitcnt lgkmcnt(0)\n" \
"s_xor_b32 s70, s62, 3\n" \
"s_lshl_b32 s70, s70, 20\n" \
"s_add_u32 s70, s70, 524288\n" \
"s_add_u32 s72, s42, s70\n" \
"s_addc_u32 s73, s43, 0\n" \
"s_add_u32 m0, s71, 12288\n" \
"s_nop 0\n" \
"global_load_lds_dwordx4 v36, s[72:73] offset:0\n" \
"global_load_lds_dwordx4 v37, s[72:73] offset:1024\n" \
"global_load_lds_dwordx4 v38, s[72:73] offset:2048\n" \
"global_load_lds_dwordx4 v39, s[72:73] offset:3072\n" \
"s_waitcnt vmcnt(28)\n" \
"ds_read_b64_tr_b16 a[64:65], v34 offset:16384\n" \
"ds_read_b64_tr_b16 a[66:67], v34 offset:16640\n" \
"ds_read_b64_tr_b16 a[68:69], v34 offset:18432\n" \
"ds_read_b64_tr_b16 a[70:71], v34 offset:18688\n" \
"ds_read_b64_tr_b16 a[72:73], v34 offset:16416\n" \
"ds_read_b64_tr_b16 a[74:75], v34 offset:16672\n" \
"ds_read_b64_tr_b16 a[76:77], v34 offset:18464\n" \
"ds_read_b64_tr_b16 a[78:79], v34 offset:18720\n" \
"s_waitcnt lgkmcnt(0)\n" \
"s_xor_b32 s70, s62, 0\n" \
"s_lshl_b32 s70, s70, 20\n" \
"s_add_u32 s70, s70, 0\n" \
"s_add_u32 s72, s44, s70\n" \
"s_addc_u32 s73, s45, 0\n" \
"s_add_u32 m0, s71, 16384\n" \
"s_nop 0\n" \
"global_load_lds_dwordx4 v36, s[72:73] offset:0\n" \
"global_load_lds_dwordx4 v37, s[72:73] offset:1024\n" \
"global_load_lds_dwordx4 v38, s[72:73] offset:2048\n" \
"global_load_lds_dwordx4 v39, s[72:73] offset:3072\n" \
"s_waitcnt vmcnt(28)\n" \
"ds_read_b64_tr_b16 a[80:81], v34 offset:20480\n" \
"ds_read_b64_tr_b16 a[82:83], v34 offset:20736\n" \
"ds_read_b64_tr_b16 a[84:85], v34 offset:22528\n" \
"ds_read_b64_tr_b16 a[86:87], v34 offset:22784\n" \
"ds_read_b64_tr_b16 a[88:89], v34 offset:20512\n" \
"ds_read_b64_tr_b16 a[90:91], v34 offset:20768\n" \
"ds_read_b64_tr_b16 a[92:93], v34 offset:22560\n" \
"ds_read_b64_tr_b16 a[94:95], v34 offset:22816\n" \
"s_waitcnt lgkmcnt(0)\n" \
"s_xor_b32 s70, s62, 0\n" \
"s_lshl_b32 s70, s70, 20\n" \
"s_add_u32 s70, s70, 524288\n" \
"s_add_u32 s72, s44, s70\n" \
"s_addc_u32 s73, s45, 0\n" \
"s_add_u32 m0, s71, 20480\n" \
"s_nop 0\n" \
"global_load_lds_dwordx4 v36, s[72:73] offset:0\n" \
"global_load_lds_dwordx4 v37, s[72:73] offset:1024\n" \
"global_load_lds_dwordx4 v38, s[72:73] offset:2048\n" \
"global_load_lds_dwordx4 v39, s[72:73] offset:3072\n" \
"s_waitcnt vmcnt(28)\n" \
"ds_read_b64_tr_b16 a[96:97], v34 offset:24576\n" \
"ds_read_b64_tr_b16 a[98:99], v34 offset:24832\n" \
"ds_read_b64_tr_b16 a[100:101], v34 offset:26624\n" \
"ds_read_b64_tr_b16 a[102:103], v34 offset:26880\n" \
"ds_read_b64_tr_b16 a[104:105], v34 offset:24608\n" \
"ds_read_b64_tr_b16 a[106:107], v34 offset:24864\n" \
"ds_read_b64_tr_b16 a[108:109], v34 offset:26656\n" \
"ds_read_b64_tr_b16 a[110:111], v34 offset:26912\n" \
"s_waitcnt lgkmcnt(0)\n" \
"s_xor_b32 s70, s62, 1\n" \
"s_lshl_b32 s70, s70, 20\n" \
"s_add_u32 s70, s70, 0\n" \
"s_add_u32 s72, s44, s70\n" \
"s_addc_u32 s73, s45, 0\n" \
"s_add_u32 m0, s71, 24576\n" \
"s_nop 0\n" \
"global_load_lds_dwordx4 v36, s[72:73] offset:0\n" \
"global_load_lds_dwordx4 v37, s[72:73] offset:1024\n" \
"global_load_lds_dwordx4 v38, s[72:73] offset:2048\n" \
"global_load_lds_dwordx4 v39, s[72:73] offset:3072\n" \
"s_waitcnt vmcnt(28)\n" \
"ds_read_b64_tr_b16 a[112:113], v34 offset:28672\n" \
"ds_read_b64_tr_b16 a[114:115], v34 offset:28928\n" \
"ds_read_b64_tr_b16 a[116:117], v34 offset:30720\n" \
"ds_read_b64_tr_b16 a[118:119], v34 offset:30976\n" \
"ds_read_b64_tr_b16 a[120:121], v34 offset:28704\n" \
"ds_read_b64_tr_b16 a[122:123], v34 offset:28960\n" \
"ds_read_b64_tr_b16 a[124:125], v34 offset:30752\n" \
"ds_read_b64_tr_b16 a[126:127], v34 offset:31008\n" \
"s_waitcnt lgkmcnt(0)\n" \
"s_xor_b32 s70, s62, 1\n" \
"s_lshl_b32 s70, s70, 20\n" \
"s_add_u32 s70, s70, 524288\n" \
"s_add_u32 s72, s44, s70\n" \
"s_addc_u32 s73, s45, 0\n" \
"s_add_u32 m0, s71, 28672\n" \
"s_nop 0\n" \
"global_load_lds_dwordx4 v36, s[72:73] offset:0\n" \
"global_load_lds_dwordx4 v37, s[72:73] offset:1024\n" \
"global_load_lds_dwordx4 v38, s[72:73] offset:2048\n" \
"global_load_lds_dwordx4 v39, s[72:73] offset:3072\n" \
"s_waitcnt vmcnt(28)\n" \
"ds_read_b64_tr_b16 a[128:129], v34 offset:0\n" \
"ds_read_b64_tr_b16 a[130:131], v34 offset:256\n" \
"ds_read_b64_tr_b16 a[132:133], v34 offset:2048\n" \
"ds_read_b64_tr_b16 a[134:135], v34 offset:2304\n" \
"ds_read_b64_tr_b16 a[136:137], v34 offset:32\n" \
"ds_read_b64_tr_b16 a[138:139], v34 offset:288\n" \
"ds_read_b64_tr_b16 a[140:141], v34 offset:2080\n" \
"ds_read_b64_tr_b16 a[142:143], v34 offset:2336\n" \
"s_waitcnt lgkmcnt(0)\n" \
"s_xor_b32 s70, s62, 2\n" \
"s_lshl_b32 s70, s70, 20\n" \
"s_add_u32 s70, s70, 0\n" \
"s_add_u32 s72, s44, s70\n" \
"s_addc_u32 s73, s45, 0\n" \
"s_add_u32 m0, s71, 0\n" \
"s_nop 0\n" \
"global_load_lds_dwordx4 v36, s[72:73] offset:0\n" \
"global_load_lds_dwordx4 v37, s[72:73] offset:1024\n" \
"global_load_lds_dwordx4 v38, s[72:73] offset:2048\n" \
"global_load_lds_dwordx4 v39, s[72:73] offset:3072\n" \
"s_waitcnt vmcnt(28)\n" \
"ds_read_b64_tr_b16 a[144:145], v34 offset:4096\n" \
"ds_read_b64_tr_b16 a[146:147], v34 offset:4352\n" \
"ds_read_b64_tr_b16 a[148:149], v34 offset:6144\n" \
"ds_read_b64_tr_b16 a[150:151], v34 offset:6400\n" \
"ds_read_b64_tr_b16 a[152:153], v34 offset:4128\n" \
"ds_read_b64_tr_b16 a[154:155], v34 offset:4384\n" \
"ds_read_b64_tr_b16 a[156:157], v34 offset:6176\n" \
"ds_read_b64_tr_b16 a[158:159], v34 offset:6432\n" \
"s_waitcnt lgkmcnt(0)\n" \
"s_xor_b32 s70, s62, 2\n" \
"s_lshl_b32 s70, s70, 20\n" \
"s_add_u32 s70, s70, 524288\n" \
"s_add_u32 s72, s44, s70\n" \
"s_addc_u32 s73, s45, 0\n" \
"s_add_u32 m0, s71, 4096\n" \
"s_nop 0\n" \
"global_load_lds_dwordx4 v36, s[72:73] offset:0\n" \
"global_load_lds_dwordx4 v37, s[72:73] offset:1024\n" \
"global_load_lds_dwordx4 v38, s[72:73] offset:2048\n" \
"global_load_lds_dwordx4 v39, s[72:73] offset:3072\n" \
"s_waitcnt vmcnt(28)\n" \
"ds_read_b64_tr_b16 a[160:161], v34 offset:8192\n" \
"ds_read_b64_tr_b16 a[162:163], v34 offset:8448\n" \
"ds_read_b64_tr_b16 a[164:165], v34 offset:10240\n" \
"ds_read_b64_tr_b16 a[166:167], v34 offset:10496\n" \
"ds_read_b64_tr_b16 a[168:169], v34 offset:8224\n" \
"ds_read_b64_tr_b16 a[170:171], v34 offset:8480\n" \
"ds_read_b64_tr_b16 a[172:173], v34 offset:10272\n" \
"ds_read_b64_tr_b16 a[174:175], v34 offset:10528\n" \
"s_waitcnt lgkmcnt(0)\n" \
"s_xor_b32 s70, s62, 3\n" \
"s_lshl_b32 s70, s70, 20\n" \
"s_add_u32 s70, s70, 0\n" \
"s_add_u32 s72, s44, s70\n" \
"s_addc_u32 s73, s45, 0\n" \
"s_add_u32 m0, s71, 8192\n" \
"s_nop 0\n" \
"global_load_lds_dwordx4 v36, s[72:73] offset:0\n" \
"global_load_lds_dwordx4 v37, s[72:73] offset:1024\n" \
"global_load_lds_dwordx4 v38, s[72:73] offset:2048\n" \
"global_load_lds_dwordx4 v39, s[72:73] offset:3072\n" \
"s_waitcnt vmcnt(28)\n" \
"ds_read_b64_tr_b16 a[176:177], v34 offset:12288\n" \
"ds_read_b64_tr_b16 a[178:179], v34 offset:12544\n" \
"ds_read_b64_tr_b16 a[180:181], v34 offset:14336\n" \
"ds_read_b64_tr_b16 a[182:183], v34 offset:14592\n" \
"ds_read_b64_tr_b16 a[184:185], v34 offset:12320\n" \
"ds_read_b64_tr_b16 a[186:187], v34 offset:12576\n" \
"ds_read_b64_tr_b16 a[188:189], v34 offset:14368\n" \
"ds_read_b64_tr_b16 a[190:191], v34 offset:14624\n" \
"s_waitcnt lgkmcnt(0)\n" \
"s_xor_b32 s70, s62, 3\n" \
"s_lshl_b32 s70, s70, 20\n" \
"s_add_u32 s70, s70, 524288\n" \
"s_add_u32 s72, s44, s70\n" \
"s_addc_u32 s73, s45, 0\n" \
"s_add_u32 m0, s71, 12288\n" \
"s_nop 0\n" \
"global_load_lds_dwordx4 v36, s[72:73] offset:0\n" \
"global_load_lds_dwordx4 v37, s[72:73] offset:1024\n" \
"global_load_lds_dwordx4 v38, s[72:73] offset:2048\n" \
"global_load_lds_dwordx4 v39, s[72:73] offset:3072\n" \
"s_waitcnt vmcnt(28)\n" \
"ds_read_b64_tr_b16 a[192:193], v34 offset:16384\n" \
"ds_read_b64_tr_b16 a[194:195], v34 offset:16640\n" \
"ds_read_b64_tr_b16 a[196:197], v34 offset:18432\n" \
"ds_read_b64_tr_b16 a[198:199], v34 offset:18688\n" \
"ds_read_b64_tr_b16 a[200:201], v34 offset:16416\n" \
"ds_read_b64_tr_b16 a[202:203], v34 offset:16672\n" \
"ds_read_b64_tr_b16 a[204:205], v34 offset:18464\n" \
"ds_read_b64_tr_b16 a[206:207], v34 offset:18720\n" \
"s_waitcnt lgkmcnt(0)\n" \
"s_add_u32 s72, s46, 0\n" \
"s_addc_u32 s73, s47, 0\n" \
"s_add_u32 m0, s71, 16384\n" \
"s_nop 0\n" \
"global_load_lds_dwordx4 v40, s[72:73] offset:0\n" \
"global_load_lds_dwordx4 v41, s[72:73] offset:1024\n" \
"global_load_lds_dwordx4 v42, s[72:73] offset:2048\n" \
"global_load_lds_dwordx4 v43, s[72:73] offset:3072\n" \
"s_waitcnt vmcnt(28)\n" \
"ds_read_b64_tr_b16 a[208:209], v34 offset:20480\n" \
"ds_read_b64_tr_b16 a[210:211], v34 offset:20736\n" \
"ds_read_b64_tr_b16 a[212:213], v34 offset:22528\n" \
"ds_read_b64_tr_b16 a[214:215], v34 offset:22784\n" \
"ds_read_b64_tr_b16 a[216:217], v34 offset:20512\n" \
"ds_read_b64_tr_b16 a[218:219], v34 offset:20768\n" \
"ds_read_b64_tr_b16 a[220:221], v34 offset:22560\n" \
"ds_read_b64_tr_b16 a[222:223], v34 offset:22816\n" \
"s_waitcnt lgkmcnt(0)\n" \
"s_add_u32 s72, s46, 65536\n" \
"s_addc_u32 s73, s47, 0\n" \
"s_add_u32 m0, s71, 20480\n" \
"s_nop 0\n" \
"global_load_lds_dwordx4 v40, s[72:73] offset:0\n" \
"global_load_lds_dwordx4 v41, s[72:73] offset:1024\n" \
"global_load_lds_dwordx4 v42, s[72:73] offset:2048\n" \
"global_load_lds_dwordx4 v43, s[72:73] offset:3072\n" \
"s_waitcnt vmcnt(28)\n" \
"ds_read_b64_tr_b16 a[224:225], v34 offset:24576\n" \
"ds_read_b64_tr_b16 a[226:227], v34 offset:24832\n" \
"ds_read_b64_tr_b16 a[228:229], v34 offset:26624\n" \
"ds_read_b64_tr_b16 a[230:231], v34 offset:26880\n" \
"ds_read_b64_tr_b16 a[232:233], v34 offset:24608\n" \
"ds_read_b64_tr_b16 a[234:235], v34 offset:24864\n" \
"ds_read_b64_tr_b16 a[236:237], v34 offset:26656\n" \
"ds_read_b64_tr_b16 a[238:239], v34 offset:26912\n" \
"s_waitcnt lgkmcnt(0)\n" \
"s_add_u32 s72, s46, 64\n" \
"s_addc_u32 s73, s47, 0\n" \
"s_add_u32 m0, s71, 24576\n" \
"s_nop 0\n" \
"global_load_lds_dwordx4 v40, s[72:73] offset:0\n" \
"global_load_lds_dwordx4 v41, s[72:73] offset:1024\n" \
"global_load_lds_dwordx4 v42, s[72:73] offset:2048\n" \
"global_load_lds_dwordx4 v43, s[72:73] offset:3072\n" \
"s_waitcnt vmcnt(28)\n" \
"ds_read_b64_tr_b16 a[240:241], v34 offset:28672\n" \
"ds_read_b64_tr_b16 a[242:243], v34 offset:28928\n" \
"ds_read_b64_tr_b16 a[244:245], v34 offset:30720\n" \
"ds_read_b64_tr_b16 a[246:247], v34 offset:30976\n" \
"ds_read_b64_tr_b16 a[248:249], v34 offset:28704\n" \
"ds_read_b64_tr_b16 a[250:251], v34 offset:28960\n" \
"ds_read_b64_tr_b16 a[252:253], v34 offset:30752\n" \
"ds_read_b64_tr_b16 a[254:255], v34 offset:31008\n" \
"s_waitcnt lgkmcnt(0)\n" \
"s_add_u32 s72, s46, 65600\n" \
"s_addc_u32 s73, s47, 0\n" \
"s_add_u32 m0, s71, 28672\n" \
"s_nop 0\n" \
"global_load_lds_dwordx4 v40, s[72:73] offset:0\n" \
"global_load_lds_dwordx4 v41, s[72:73] offset:1024\n" \
"global_load_lds_dwordx4 v42, s[72:73] offset:2048\n" \
"global_load_lds_dwordx4 v43, s[72:73] offset:3072\n" \
"s_waitcnt vmcnt(28)\n" \
"ds_read_b64_tr_b16 v[128:129], v34 offset:0\n" \
"ds_read_b64_tr_b16 v[130:131], v34 offset:256\n" \
"ds_read_b64_tr_b16 v[132:133], v34 offset:2048\n" \
"ds_read_b64_tr_b16 v[134:135], v34 offset:2304\n" \
"ds_read_b64_tr_b16 v[136:137], v34 offset:32\n" \
"ds_read_b64_tr_b16 v[138:139], v34 offset:288\n" \
"ds_read_b64_tr_b16 v[140:141], v34 offset:2080\n" \
"ds_read_b64_tr_b16 v[142:143], v34 offset:2336\n" \
"s_waitcnt lgkmcnt(0)\n" \
"s_xor_b32 s70, s62, 0\n" \
"s_lshl_b32 s70, s70, 20\n" \
"s_add_u32 s70, s70, 0\n" \
"s_add_u32 s72, s40, s70\n" \
"s_addc_u32 s73, s41, 0\n" \
"s_add_u32 m0, s71, 0\n" \
"s_nop 0\n" \
"global_load_lds_dwordx4 v36, s[72:73] offset:0\n" \
"global_load_lds_dwordx4 v37, s[72:73] offset:1024\n" \
"global_load_lds_dwordx4 v38, s[72:73] offset:2048\n" \
"global_load_lds_dwordx4 v39, s[72:73] offset:3072\n" \
"s_waitcnt vmcnt(28)\n" \
"ds_read_b64_tr_b16 v[144:145], v34 offset:4096\n" \
"ds_read_b64_tr_b16 v[146:147], v34 offset:4352\n" \
"ds_read_b64_tr_b16 v[148:149], v34 offset:6144\n" \
"ds_read_b64_tr_b16 v[150:151], v34 offset:6400\n" \
"ds_read_b64_tr_b16 v[152:153], v34 offset:4128\n" \
"ds_read_b64_tr_b16 v[154:155], v34 offset:4384\n" \
"ds_read_b64_tr_b16 v[156:157], v34 offset:6176\n" \
"ds_read_b64_tr_b16 v[158:159], v34 offset:6432\n" \
"s_waitcnt lgkmcnt(0)\n" \
"s_xor_b32 s70, s62, 0\n" \
"s_lshl_b32 s70, s70, 20\n" \
"s_add_u32 s70, s70, 0\n" \
"s_add_u32 s72, s42, s70\n" \
"s_addc_u32 s73, s43, 0\n" \
"s_add_u32 m0, s71, 4096\n" \
"s_nop 0\n" \
"global_load_lds_dwordx4 v36, s[72:73] offset:0\n" \
"global_load_lds_dwordx4 v37, s[72:73] offset:1024\n" \
"global_load_lds_dwordx4 v38, s[72:73] offset:2048\n" \
"global_load_lds_dwordx4 v39, s[72:73] offset:3072\n" \
"s_waitcnt vmcnt(28)\n" \
"ds_read_b64_tr_b16 v[160:161], v34 offset:8192\n" \
"ds_read_b64_tr_b16 v[162:163], v34 offset:8448\n" \
"ds_read_b64_tr_b16 v[164:165], v34 offset:10240\n" \
"ds_read_b64_tr_b16 v[166:167], v34 offset:10496\n" \
"ds_read_b64_tr_b16 v[168:169], v34 offset:8224\n" \
"ds_read_b64_tr_b16 v[170:171], v34 offset:8480\n" \
"ds_read_b64_tr_b16 v[172:173], v34 offset:10272\n" \
"ds_read_b64_tr_b16 v[174:175], v34 offset:10528\n" \
"s_waitcnt lgkmcnt(0)\n" \
"s_xor_b32 s70, s62, 0\n" \
"s_lshl_b32 s70, s70, 20\n" \
"s_add_u32 s70, s70, 524288\n" \
"s_add_u32 s72, s40, s70\n" \
"s_addc_u32 s73, s41, 0\n" \
"s_add_u32 m0, s71, 8192\n" \
"s_nop 0\n" \
"global_load_lds_dwordx4 v36, s[72:73] offset:0\n" \
"global_load_lds_dwordx4 v37, s[72:73] offset:1024\n" \
"global_load_lds_dwordx4 v38, s[72:73] offset:2048\n" \
"global_load_lds_dwordx4 v39, s[72:73] offset:3072\n" \
"s_waitcnt vmcnt(28)\n" \
"ds_read_b64_tr_b16 v[176:177], v34 offset:12288\n" \
"ds_read_b64_tr_b16 v[178:179], v34 offset:12544\n" \
"ds_read_b64_tr_b16 v[180:181], v34 offset:14336\n" \
"ds_read_b64_tr_b16 v[182:183], v34 offset:14592\n" \
"ds_read_b64_tr_b16 v[184:185], v34 offset:12320\n" \
"ds_read_b64_tr_b16 v[186:187], v34 offset:12576\n" \
"ds_read_b64_tr_b16 v[188:189], v34 offset:14368\n" \
"ds_read_b64_tr_b16 v[190:191], v34 offset:14624\n" \
"s_waitcnt lgkmcnt(0)\n" \
"s_xor_b32 s70, s62, 3\n" \
"s_lshl_b32 s70, s70, 20\n" \
"s_add_u32 s70, s70, 524288\n" \
"s_add_u32 s72, s40, s70\n" \
"s_addc_u32 s73, s41, 0\n" \
"s_add_u32 m0, s71, 12288\n" \
"s_nop 0\n" \
"global_load_lds_dwordx4 v36, s[72:73] offset:0\n" \
"global_load_lds_dwordx4 v37, s[72:73] offset:1024\n" \
"global_load_lds_dwordx4 v38, s[72:73] offset:2048\n" \
"global_load_lds_dwordx4 v39, s[72:73] offset:3072\n" \
"s_waitcnt vmcnt(28)\n" \
"ds_read_b64_tr_b16 v[192:193], v34 offset:16384\n" \
"ds_read_b64_tr_b16 v[194:195], v34 offset:16640\n" \
"ds_read_b64_tr_b16 v[196:197], v34 offset:18432\n" \
"ds_read_b64_tr_b16 v[198:199], v34 offset:18688\n" \
"ds_read_b64_tr_b16 v[200:201], v34 offset:16416\n" \
"ds_read_b64_tr_b16 v[202:203], v34 offset:16672\n" \
"ds_read_b64_tr_b16 v[204:205], v34 offset:18464\n" \
"ds_read_b64_tr_b16 v[206:207], v34 offset:18720\n" \
"s_waitcnt lgkmcnt(0)\n" \
"s_waitcnt vmcnt(24)\n" \
"ds_read_b64_tr_b16 v[208:209], v34 offset:20480\n" \
"ds_read_b64_tr_b16 v[210:211], v34 offset:20736\n" \
"ds_read_b64_tr_b16 v[212:213], v34 offset:22528\n" \
"ds_read_b64_tr_b16 v[214:215], v34 offset:22784\n" \
"ds_read_b64_tr_b16 v[216:217], v34 offset:20512\n" \
"ds_read_b64_tr_b16 v[218:219], v34 offset:20768\n" \
"ds_read_b64_tr_b16 v[220:221], v34 offset:22560\n" \
"ds_read_b64_tr_b16 v[222:223], v34 offset:22816\n" \
"s_waitcnt lgkmcnt(0)\n" \
"s_waitcnt vmcnt(20)\n" \
"ds_read_b64_tr_b16 v[224:225], v34 offset:24576\n" \
"ds_read_b64_tr_b16 v[226:227], v34 offset:24832\n" \
"ds_read_b64_tr_b16 v[228:229], v34 offset:26624\n" \
"ds_read_b64_tr_b16 v[230:231], v34 offset:26880\n" \
"ds_read_b64_tr_b16 v[232:233], v34 offset:24608\n" \
"ds_read_b64_tr_b16 v[234:235], v34 offset:24864\n" \
"ds_read_b64_tr_b16 v[236:237], v34 offset:26656\n" \
"ds_read_b64_tr_b16 v[238:239], v34 offset:26912\n" \
"s_waitcnt lgkmcnt(0)\n" \
"s_waitcnt vmcnt(16)\n" \
"ds_read_b64_tr_b16 v[240:241], v34 offset:28672\n" \
"ds_read_b64_tr_b16 v[242:243], v34 offset:28928\n" \
"ds_read_b64_tr_b16 v[244:245], v34 offset:30720\n" \
"ds_read_b64_tr_b16 v[246:247], v34 offset:30976\n" \
"ds_read_b64_tr_b16 v[248:249], v34 offset:28704\n" \
"ds_read_b64_tr_b16 v[250:251], v34 offset:28960\n" \
"ds_read_b64_tr_b16 v[252:253], v34 offset:30752\n" \
"ds_read_b64_tr_b16 v[254:255], v34 offset:31008\n" \
"s_waitcnt lgkmcnt(0)\n" \
"s_waitcnt vmcnt(12)\n" \
"ds_read_b64_tr_b16 v[56:57], v34 offset:0\n" \
"ds_read_b64_tr_b16 v[58:59], v34 offset:256\n" \
"ds_read_b64_tr_b16 v[60:61], v34 offset:2048\n" \
"ds_read_b64_tr_b16 v[62:63], v34 offset:2304\n" \
"ds_read_b64_tr_b16 v[64:65], v34 offset:32\n" \
"ds_read_b64_tr_b16 v[66:67], v34 offset:288\n" \
"ds_read_b64_tr_b16 v[68:69], v34 offset:2080\n" \
"ds_read_b64_tr_b16 v[70:71], v34 offset:2336\n" \
"s_waitcnt lgkmcnt(0)\n" \
"ds_write_b128 v113, v[56:59] offset:0\n" \
"ds_write_b128 v113, v[60:63] offset:1024\n" \
"ds_write_b128 v113, v[64:67] offset:2048\n" \
"ds_write_b128 v113, v[68:71] offset:3072\n" \
"s_waitcnt lgkmcnt(0)\n" \
"s_waitcnt vmcnt(8)\n" \
"ds_read_b64_tr_b16 v[56:57], v34 offset:4096\n" \
"ds_read_b64_tr_b16 v[58:59], v34 offset:4352\n" \
"ds_read_b64_tr_b16 v[60:61], v34 offset:6144\n" \
"ds_read_b64_tr_b16 v[62:63], v34 offset:6400\n" \
"ds_read_b64_tr_b16 v[64:65], v34 offset:4128\n" \
"ds_read_b64_tr_b16 v[66:67], v34 offset:4384\n" \
"ds_read_b64_tr_b16 v[68:69], v34 offset:6176\n" \
"ds_read_b64_tr_b16 v[70:71], v34 offset:6432\n" \
"s_waitcnt lgkmcnt(0)\n" \
"ds_write_b128 v113, v[56:59] offset:4096\n" \
"ds_write_b128 v113, v[60:63] offset:5120\n" \
"ds_write_b128 v113, v[64:67] offset:6144\n" \
"ds_write_b128 v113, v[68:71] offset:7168\n" \
"s_waitcnt lgkmcnt(0)\n" \
"s_waitcnt vmcnt(4)\n" \
"ds_read_b64_tr_b16 v[56:57], v34 offset:8192\n" \
"ds_read_b64_tr_b16 v[58:59], v34 offset:8448\n" \
"ds_read_b64_tr_b16 v[60:61], v34 offset:10240\n" \
"ds_read_b64_tr_b16 v[62:63], v34 offset:10496\n" \
"ds_read_b64_tr_b16 v[64:65], v34 offset:8224\n" \
"ds_read_b64_tr_b16 v[66:67], v34 offset:8480\n" \
"ds_read_b64_tr_b16 v[68:69], v34 offset:10272\n" \
"ds_read_b64_tr_b16 v[70:71], v34 offset:10528\n" \
"s_waitcnt lgkmcnt(0)\n" \
"ds_write_b128 v113, v[56:59] offset:8192\n" \
"ds_write_b128 v113, v[60:63] offset:9216\n" \
"ds_write_b128 v113, v[64:67] offset:10240\n" \
"ds_write_b128 v113, v[68:71] offset:11264\n" \
"s_waitcnt lgkmcnt(0)\n" \
"s_waitcnt vmcnt(0)\n" \
"ds_read_b64_tr_b16 v[56:57], v34 offset:12288\n" \
"ds_read_b64_tr_b16 v[58:59], v34 offset:12544\n" \
"ds_read_b64_tr_b16 v[60:61], v34 offset:14336\n" \
"ds_read_b64_tr_b16 v[62:63], v34 offset:14592\n" \
"ds_read_b64_tr_b16 v[64:65], v34 offset:12320\n" \
"ds_read_b64_tr_b16 v[66:67], v34 offset:12576\n" \
"ds_read_b64_tr_b16 v[68:69], v34 offset:14368\n" \
"ds_read_b64_tr_b16 v[70:71], v34 offset:14624\n" \
"s_waitcnt lgkmcnt(0)\n" \
"ds_write_b128 v113, v[56:59] offset:12288\n" \
"ds_write_b128 v113, v[60:63] offset:13312\n" \
"ds_write_b128 v113, v[64:67] offset:14336\n" \
"ds_write_b128 v113, v[68:71] offset:15360\n" \
"s_waitcnt lgkmcnt(0)\n" \
"s_barrier\n" \
"s_mov_b32 s40, 0xff00ff00\n" \
"s_mov_b32 s41, 0xff00ff00\n" \
"s_mov_b32 s44, 0x3e0293ee\n" \
"s_mov_b32 s45, 0\n" \
"s_mov_b32 s46, 200000\n" \
"s_mov_b32 s47, 49152\n" \
"s_mov_b32 s42, 2\n" \
"Lkv0%=:\n" \
"v_mov_b32 v29, 0\n" \
"global_load_dwordx2 v[30:31], v29, s[36:37] sc1\n" \
"s_waitcnt vmcnt(0)\n" \
"v_readfirstlane_b32 s74, v30\n" \
"v_readfirstlane_b32 s75, v31\n" \
"s_sub_u32 s74, s74, s63\n" \
"s_cmp_gt_u32 s74, 64\n" \
"s_cselect_b32 s74, 0, s74\n" \
"s_sub_u32 s75, s75, s63\n" \
"s_cmp_gt_u32 s75, 64\n" \
"s_cselect_b32 s75, 0, s75\n" \
"s_lshl_b32 s74, s74, 1\n" \
"s_lshl1_add_u32 s75, s75, 1\n" \
"s_min_u32 s67, s74, s75\n" \
"s_cmp_lt_u32 s42, s67\n" \
"s_cbranch_scc1 Lkv0ok%=\n" \
"s_sub_u32 s46, s46, 1\n" \
"s_cbranch_scc1 Lend%=\n" \
"s_sleep 2\n" \
"s_branch Lkv0%=\n" \
"Lkv0ok%=:\n" \
"s_lshl_b32 s35, s62, 12\n" \
"s_add_u32 s35, s35, 49152\n" \
"s_mov_b32 m0, s35\n" \
"s_nop 0\n" \
"global_load_lds_dwordx4 v114, s[56:57] offset:0 sc1\n" \
"global_load_lds_dwordx4 v114, s[56:57] offset:1024 sc1\n" \
"global_load_lds_dwordx4 v114, s[56:57] offset:2048 sc1\n" \
"global_load_lds_dwordx4 v114, s[56:57] offset:3072 sc1\n" \
"s_add_u32 s56, s56, 524288\n" \
"s_addc_u32 s57, s57, 0\n" \
"s_add_u32 s35, s35, 0x8000\n" \
"s_cmp_ge_u32 s35, 163840\n" \
"s_cselect_b32 s42, 0x20000, 0\n" \
"s_sub_u32 s35, s35, s42\n" \
"s_mov_b32 m0, s35\n" \
"s_nop 0\n" \
"global_load_lds_dwordx4 v114, s[56:57] offset:0 sc1\n" \
"global_load_lds_dwordx4 v114, s[56:57] offset:1024 sc1\n" \
"global_load_lds_dwordx4 v114, s[56:57] offset:2048 sc1\n" \
"global_load_lds_dwordx4 v114, s[56:57] offset:3072 sc1\n" \
"s_add_u32 s56, s56, 524288\n" \
"s_addc_u32 s57, s57, 0\n" \
"s_add_u32 s35, s35, 0x8000\n" \
"s_cmp_ge_u32 s35, 163840\n" \
"s_cselect_b32 s42, 0x20000, 0\n" \
"s_sub_u32 s35, s35, s42\n" \
"s_mov_b32 m0, s35\n" \
"Lhs%=:\n" \
"global_load_dword v44, v35, s[58:59] sc1\n" \
"s_waitcnt vmcnt(0)\n" \
"v_cmp_ne_u32 vcc, s63, v44\n" \
"s_cbranch_vccz Lhsd%=\n" \
"s_sub_u32 s46, s46, 1\n" \
"s_cbranch_scc1 Lend%=\n" \
"s_sleep 2\n" \
"s_branch Lhs%=\n" \
"Lhsd%=:\n" \
"s_mov_b32 s46, 200000\n" \
"s_lshr_b32 s42, s66, 0\n" \
"s_mov_b64 s[58:59], s[52:53]\n" \
"s_add_u32 s72, s52, s42\n" \
"s_addc_u32 s73, s53, 0\n" \
"s_mov_b32 s66, 340\n" \
"ds_read_b128 v[56:59], v113 offset:0\n" \
"ds_read_b128 v[60:63], v113 offset:1024\n" \
"ds_read_b128 v[64:67], v113 offset:2048\n" \
"ds_read_b128 v[68:71], v113 offset:3072\n" \
"ds_read_b128 v[72:75], v113 offset:4096\n" \
"ds_read_b128 v[76:79], v113 offset:5120\n" \
"ds_read_b128 v[80:83], v113 offset:6144\n" \
"ds_read_b128 v[84:87], v113 offset:7168\n" \
"ds_read_b128 v[88:91], v113 offset:8192\n" \
"ds_read_b128 v[96:99], v113 offset:10240\n" \
"ds_read_b128 v[100:103], v113 offset:11264\n" \
"ds_read_b128 v[92:95], v113 offset:9216\n" \
"v_mov_b32 v32, 0\n" \
"v_mov_b32 v33, 0\n" \
"v_mov_b32 v34, 0\n" \
"v_mov_b32 v35, 0\n" \
"v_mov_b32 v36, 0\n" \
"v_mov_b32 v37, 0\n" \
"v_mov_b32 v38, 0\n" \
"v_mov_b32 v39, 0\n" \
"v_mov_b32 v40, 0\n" \
"v_mov_b32 v41, 0\n" \
"v_mov_b32 v42, 0\n" \
"v_mov_b32 v43, 0\n" \
"v_mov_b32 v44, 0\n" \
"v_mov_b32 v45, 0\n" \
"v_mov_b32 v46, 0\n" \
"v_mov_b32 v47, 0\n" \
"v_mov_b32 v48, 0\n" \
"v_mov_b32 v49, 0\n" \
"v_mov_b32 v50, 0\n" \
"v_mov_b32 v51, 0\n" \
"v_mov_b32 v52, 0\n" \
"v_mov_b32 v53, 0\n" \
"v_mov_b32 v54, 0\n" \
"v_mov_b32 v55, 0\n" \
"v_add_u32 v127, s47, v114\n" \
"s_waitcnt lgkmcnt(0)\n" \
"v_mov_b32 v24, -1\n" \
"v_mov_b32 v25, -1\n" \
"v_mov_b32 v26, -1\n" \
"v_mov_b32 v27, -1\n" \
"s_xor_b32 s68, s68, 0x100000\n" \
"Lstep%=:\n" \
"ds_write_b128 v115, v[0:3]\n" \
"ds_write_b128 v115, v[4:7] offset:1024\n" \
"v_smfmac_f32_16x16x64_bf16 v[32:35], v[0:3], v[56:63], v112\n" \
"s_add_u32 s52, s52, 0x8000\n" \
"s_and_b32 s42, s52, 0x78000\n" \
"s_cselect_b32 s42, 0, 0x4000\n" \
"s_xor_b32 s52, s52, s42\n" \
"v_smfmac_f32_16x16x64_bf16 v[36:39], v[0:3], v[64:71], v112\n" \
"s_add_u32 s42, s62, 1\n" \
"s_bitcmp1_b32 s42, 1\n" \
"s_cselect_b64 exec, -1, 0\n" \
"global_store_dwordx4 v114, v[16:19], s[54:55] sc1\n" \
"s_mov_b64 exec, -1\n" \
"s_add_u32 s54, s54, 131072\n" \
"s_addc_u32 s55, s55, 0\n" \
"v_smfmac_f32_16x16x64_bf16 v[40:43], v[0:3], v[72:79], v112\n" \
"global_store_dwordx4 v114, v[24:27], s[68:69] sc1\n" \
"s_xor_b32 s68, s68, 0x100000\n" \
"v_smfmac_f32_16x16x64_bf16 v[44:47], v[0:3], v[80:87], v112\n" \
"v_smfmac_f32_16x16x64_bf16 v[32:35], v[4:7], v[88:95], v112\n" \
"s_waitcnt lgkmcnt(0)\n" \
"s_barrier\n" \
"s_memrealtime s[70:71]\n" \
"ds_read_b128 v[8:11], v116 offset:0\n" \
"ds_read_b128 v[12:15], v116 offset:1024\n" \
"v_smfmac_f32_16x16x64_bf16 v[36:39], v[4:7], v[96:103], v112\n" \
"ds_read_b128 v[16:19], v117 offset:0\n" \
"ds_read_b128 v[20:23], v117 offset:1024\n" \
"v_smfmac_f32_16x16x64_bf16 v[40:43], v[4:7], a[80:87], v112\n" \
"ds_read_b128 v[24:27], v118 offset:0\n" \
"ds_read_b128 v[28:31], v118 offset:1024\n" \
"v_smfmac_f32_16x16x64_bf16 v[44:47], v[4:7], a[88:95], v112\n" \
"ds_read_b128 v[56:59], v127 offset:0\n" \
"ds_read_b128 v[60:63], v127 offset:1024\n" \
"v_smfmac_f32_16x16x64_bf16 v[48:51], v[0:3], a[192:199], v112\n" \
"ds_read_b128 v[64:67], v127 offset:2048\n" \
"ds_read_b128 v[68:71], v127 offset:3072\n" \
"v_smfmac_f32_16x16x64_bf16 v[52:55], v[0:3], a[200:207], v112\n" \
"v_smfmac_f32_16x16x64_bf16 v[48:51], v[4:7], a[208:215], v112\n" \
"ds_read_b128 v[88:91], v113 offset:12288\n" \
"ds_read_b128 v[92:95], v113 offset:13312\n" \
"v_smfmac_f32_16x16x64_bf16 v[52:55], v[4:7], a[216:223], v112\n" \
"ds_read_b128 v[96:99], v113 offset:14336\n" \
"ds_read_b128 v[100:103], v113 offset:15360\n" \
"s_waitcnt lgkmcnt(13)\n" \
"v_smfmac_f32_16x16x64_bf16 v[32:35], v[8:11], a[0:7], v112\n" \
"ds_read_b128 v[72:75], v127 offset:4096\n" \
"v_smfmac_f32_16x16x64_bf16 v[36:39], v[8:11], a[8:15], v112\n" \
"global_load_lds_dwordx4 v114, s[56:57] offset:0 sc1\n" \
"s_waitcnt lgkmcnt(13)\n" \
"v_smfmac_f32_16x16x64_bf16 v[32:35], v[12:15], a[16:23], v112\n" \
"ds_read_b128 v[76:79], v127 offset:5120\n" \
"v_smfmac_f32_16x16x64_bf16 v[36:39], v[12:15], a[24:31], v112\n" \
"global_load_lds_dwordx4 v114, s[56:57] offset:1024 sc1\n" \
"s_waitcnt lgkmcnt(13)\n" \
"v_smfmac_f32_16x16x64_bf16 v[32:35], v[16:19], a[32:39], v112\n" \
"ds_read_b128 v[80:83], v127 offset:6144\n" \
"v_smfmac_f32_16x16x64_bf16 v[36:39], v[16:19], a[40:47], v112\n" \
"global_load_lds_dwordx4 v114, s[56:57] offset:2048 sc1\n" \
"s_waitcnt lgkmcnt(13)\n" \
"v_smfmac_f32_16x16x64_bf16 v[32:35], v[20:23], a[48:55], v112\n" \
"ds_read_b128 v[84:87], v127 offset:7168\n" \
"v_smfmac_f32_16x16x64_bf16 v[36:39], v[20:23], a[56:63], v112\n" \
"global_load_lds_dwordx4 v114, s[56:57] offset:3072 sc1\n" \
"s_waitcnt lgkmcnt(13)\n" \
"v_smfmac_f32_16x16x64_bf16 v[32:35], v[24:27], a[64:71], v112\n" \
"s_sub_u32 s42, s47, 8192\n" \
"v_add_u32 v127, s42, v115\n" \
"v_smfmac_f32_16x16x64_bf16 v[36:39], v[24:27], a[72:79], v112\n" \
"s_waitcnt lgkmcnt(4)\n" \
"v_smfmac_f32_16x16x64_bf16 v[32:35], v[28:31], v[88:95], v112\n" \
"v_smfmac_f32_16x16x64_bf16 v[36:39], v[28:31], v[96:103], v112\n" \
"v_smfmac_f32_16x16x64_bf16 v[40:43], v[8:11], a[96:103], v112\n" \
"v_add_f32 v32, v32, v34\n" \
"v_add_f32 v33, v33, v35\n" \
"v_smfmac_f32_16x16x64_bf16 v[44:47], v[8:11], a[104:111], v112\n" \
"v_add_f32 v36, v36, v38\n" \
"v_add_f32 v37, v37, v39\n" \
"v_smfmac_f32_16x16x64_bf16 v[40:43], v[12:15], a[112:119], v112\n" \
"v_cvt_pk_bf16_f32 v32, v32, v33\n" \
"v_cvt_pk_bf16_f32 v33, v36, v37\n" \
"ds_write2_b32 v119, v32, v33 offset0:0 offset1:64\n" \
"v_smfmac_f32_16x16x64_bf16 v[44:47], v[12:15], a[120:127], v112\n" \
"s_lshr_b32 s42, s66, 2\n" \
"s_add_u32 s42, s42, s70\n" \
"v_mov_b32 v34, s42\n" \
"v_mov_b32 v35, s45\n" \
"v_smfmac_f32_16x16x64_bf16 v[40:43], v[16:19], a[128:135], v112\n" \
"s_cmp_eq_u32 s62, 1\n" \
"s_cselect_b64 exec, 255, 0\n" \
"global_store_dwordx4 v114, v[34:37], s[72:73] sc1\n" \
"s_mov_b64 exec, -1\n" \
"v_smfmac_f32_16x16x64_bf16 v[44:47], v[16:19], a[136:143], v112\n" \
"v_smfmac_f32_16x16x64_bf16 v[40:43], v[20:23], a[144:151], v112\n" \
"v_smfmac_f32_16x16x64_bf16 v[44:47], v[20:23], a[152:159], v112\n" \
"s_waitcnt lgkmcnt(0)\n" \
"s_barrier\n" \
"ds_read_b64_tr_b16 v[88:89], v120 offset:0\n" \
"ds_read_b64_tr_b16 v[90:91], v120 offset:64\n" \
"v_smfmac_f32_16x16x64_bf16 v[40:43], v[24:27], a[160:167], v112\n" \
"ds_read_b64_tr_b16 v[92:93], v120 offset:1024\n" \
"ds_read_b64_tr_b16 v[94:95], v120 offset:1088\n" \
"v_smfmac_f32_16x16x64_bf16 v[44:47], v[24:27], a[168:175], v112\n" \
"ds_read_b64_tr_b16 v[96:97], v120 offset:256\n" \
"ds_read_b64_tr_b16 v[98:99], v120 offset:320\n" \
"v_smfmac_f32_16x16x64_bf16 v[40:43], v[28:31], a[176:183], v112\n" \
"ds_read_b64_tr_b16 v[100:101], v120 offset:1280\n" \
"ds_read_b64_tr_b16 v[102:103], v120 offset:1344\n" \
"v_smfmac_f32_16x16x64_bf16 v[44:47], v[28:31], a[184:191], v112\n" \
"v_smfmac_f32_16x16x64_bf16 v[48:51], v[8:11], a[224:231], v112\n" \
"v_add_f32 v40, v40, v42\n" \
"v_add_f32 v41, v41, v43\n" \
"v_smfmac_f32_16x16x64_bf16 v[52:55], v[8:11], a[232:239], v112\n" \
"v_add_f32 v44, v44, v46\n" \
"v_add_f32 v45, v45, v47\n" \
"v_smfmac_f32_16x16x64_bf16 v[48:51], v[12:15], a[240:247], v112\n" \
"v_cvt_pk_bf16_f32 v40, v40, v41\n" \
"v_cvt_pk_bf16_f32 v41, v44, v45\n" \
"ds_write_b64 v121, v[40:41]\n" \
"s_waitcnt lgkmcnt(7)\n" \
"v_mfma_f32_16x16x32_bf16 v[32:35], v[56:59], v[88:91], 0\n" \
"s_waitcnt lgkmcnt(5)\n" \
"v_mfma_f32_16x16x32_bf16 v[32:35], v[60:63], v[92:95], v[32:35]\n" \
"s_waitcnt lgkmcnt(3)\n" \
"v_mfma_f32_16x16x32_bf16 v[32:35], v[64:67], v[96:99], v[32:35]\n" \
"s_waitcnt lgkmcnt(1)\n" \
"v_mfma_f32_16x16x32_bf16 v[32:35], v[68:71], v[100:103], v[32:35]\n" \
"v_mfma_f32_16x16x32_bf16 v[36:39], v[72:75], v[88:91], 0\n" \
"s_waitcnt lgkmcnt(0)\n" \
"s_barrier\n" \
"ds_read_b64_tr_b16 v[56:57], v122 offset:0\n" \
"ds_read_b64_tr_b16 v[58:59], v122 offset:32\n" \
"v_mfma_f32_16x16x32_bf16 v[36:39], v[76:79], v[92:95], v[36:39]\n" \
"ds_read_b64_tr_b16 v[60:61], v122 offset:256\n" \
"ds_read_b64_tr_b16 v[62:63], v122 offset:288\n" \
"v_mfma_f32_16x16x32_bf16 v[36:39], v[80:83], v[96:99], v[36:39]\n" \
"ds_read_b128 v[64:67], v127 offset:0\n" \
"ds_read_b128 v[68:71], v127 offset:1024\n" \
"v_mfma_f32_16x16x32_bf16 v[36:39], v[84:87], v[100:103], v[36:39]\n" \
"v_max3_f32 v78, v32, v33, v34\n" \
"v_smfmac_f32_16x16x64_bf16 v[52:55], v[12:15], a[248:255], v112\n" \
"v_smfmac_f32_16x16x64_bf16 v[48:51], v[16:19], v[128:135], v112\n" \
"v_max3_f32 v78, v78, v35, v36\n" \
"v_max3_f32 v78, v78, v37, v38\n" \
"v_smfmac_f32_16x16x64_bf16 v[52:55], v[16:19], v[136:143], v112\n" \
"v_max_f32 v78, v78, v39\n" \
"v_cndmask_b32 v32, v32, v36, s[40:41]\n" \
"s_waitcnt lgkmcnt(4)\n" \
"v_mfma_f32_16x16x32_bf16 v[40:43], v[56:59], v[88:91], 0\n" \
"v_cndmask_b32 v33, v33, v37, s[40:41]\n" \
"v_mfma_f32_16x16x32_bf16 v[44:47], v[56:59], v[96:99], 0\n" \
"s_waitcnt lgkmcnt(2)\n" \
"v_mfma_f32_16x16x32_bf16 v[40:43], v[60:63], v[92:95], v[40:43]\n" \
"v_cndmask_b32 v34, v34, v38, s[40:41]\n" \
"v_mfma_f32_16x16x32_bf16 v[44:47], v[60:63], v[100:103], v[44:47]\n" \
"v_cndmask_b32 v35, v35, v39, s[40:41]\n" \
"v_smfmac_f32_16x16x64_bf16 v[48:51], v[20:23], v[144:151], v112\n" \
"s_waitcnt vmcnt(4)\n" \
"v_mov_b32 v92, s70\n" \
"v_mov_b32 v93, -1\n" \
"v_lshlrev_b32 v94, 3, v114\n" \
"v_smfmac_f32_16x16x64_bf16 v[52:55], v[20:23], v[152:159], v112\n" \
"s_cmp_eq_u32 s62, 0\n" \
"s_cselect_b64 exec, -1, 0\n" \
"global_load_dwordx2 v[92:93], v94, s[58:59] sc1\n" \
"s_mov_b64 exec, -1\n" \
"v_add_f32 v40, v40, v46\n" \
"v_add_f32 v41, v41, v47\n" \
"v_max3_f32 v79, v78, v40, v41\n" \
"v_max3_f32 v78, v78, v40, v41\n" \
"v_smfmac_f32_16x16x64_bf16 v[48:51], v[24:27], v[160:167], v112\n" \
"s_nop 0\n" \
"v_permlane16_swap_b32 v78, v79\n" \
"v_max_f32 v78, v78, v79\n" \
"v_mov_b32 v79, v78\n" \
"v_smfmac_f32_16x16x64_bf16 v[52:55], v[24:27], v[168:175], v112\n" \
"s_nop 0\n" \
"v_permlane32_swap_b32 v78, v79\n" \
"v_max_f32 v78, v78, v79\n" \
"v_mul_f32 v78, s44, v78\n" \
"v_smfmac_f32_16x16x64_bf16 v[48:51], v[28:31], v[176:183], v112\n" \
"v_fma_f32 v32, v32, s44, -v78\n" \
"v_fma_f32 v33, v33, s44, -v78\n" \
"v_smfmac_f32_16x16x64_bf16 v[52:55], v[28:31], v[184:191], v112\n" \
"v_fma_f32 v34, v34, s44, -v78\n" \
"v_fma_f32 v35, v35, s44, -v78\n" \
"v_exp_f32 v32, v32\n" \
"v_exp_f32 v33, v33\n" \
"v_exp_f32 v34, v34\n" \
"v_exp_f32 v35, v35\n" \
"v_cvt_pk_bf16_f32 v72, v32, v33\n" \
"v_fma_f32 v40, v40, s44, -v78\n" \
"v_cvt_pk_bf16_f32 v73, v34, v35\n" \
"v_fma_f32 v41, v41, s44, -v78\n" \
"v_mov_b32_dpp v74, v72 row_ror:8 row_mask:0xf bank_mask:0xf\n" \
"v_mov_b32_dpp v75, v73 row_ror:8 row_mask:0xf bank_mask:0xf\n" \
"v_exp_f32 v40, v40\n" \
"v_exp_f32 v41, v41\n" \
"s_waitcnt lgkmcnt(0)\n" \
"v_mfma_f32_16x16x32_bf16 v[88:91], v[104:107], v[72:75], 0\n" \
"v_cvt_pk_bf16_f32 v79, v40, v41\n" \
"v_add_f32 v48, v48, v50\n" \
"v_mfma_f32_16x16x32_bf16 v[80:83], v[64:67], v[72:75], 0\n" \
"v_cndmask_b32 v76, v79, 0, s[40:41]\n" \
"v_cndmask_b32 v77, 0, v79, s[40:41]\n" \
"v_mfma_f32_16x16x32_bf16 v[84:87], v[68:71], v[72:75], 0\n" \
"v_add_f32 v49, v49, v51\n" \
"v_add_f32 v52, v52, v54\n" \
"v_mfma_f32_16x16x16_bf16 v[88:91], v[104:105], v[76:77], v[88:91]\n" \
"v_add_f32 v53, v53, v55\n" \
"v_cvt_pk_bf16_f32 v48, v48, v49\n" \
"v_cvt_pk_bf16_f32 v49, v52, v53\n" \
"v_mov_b32 v0, 0\n" \
"s_nop 0\n" \
"v_mfma_f32_16x16x16_bf16 v[80:83], v[48:49], v[76:77], v[80:83]\n" \
"ds_write2_b32 v119, v48, v49 offset0:0 offset1:64\n" \
"s_cmp_eq_u32 s45, 63\n" \
"v_mfma_f32_16x16x16_bf16 v[84:87], v[48:49], v[76:77], v[84:87]\n" \
"s_cbranch_scc1 Lfin%=\n" \
"v_rcp_f32 v88, v88\n" \
"v_mov_b32 v1, 0\n" \
"v_mov_b32 v2, 0\n" \
"v_mov_b32 v3, 0\n" \
"v_mov_b32 v4, 0\n" \
"v_mov_b32 v5, 0\n" \
"s_nop 0\n" \
"v_cndmask_b32 v80, v80, v84, s[40:41]\n" \
"v_cndmask_b32 v81, v81, v85, s[40:41]\n" \
"v_cndmask_b32 v82, v82, v86, s[40:41]\n" \
"v_cndmask_b32 v83, v83, v87, s[40:41]\n" \
"v_mul_f32 v80, v80, v88\n" \
"v_mul_f32 v81, v81, v88\n" \
"v_mul_f32 v82, v82, v88\n" \
"v_mul_f32 v83, v83, v88\n" \
"v_cvt_pk_bf16_f32 v80, v80, v81\n" \
"v_cvt_pk_bf16_f32 v81, v82, v83\n" \
"ds_write_b64 v123, v[80:81]\n" \
"v_mov_b32 v6, 0\n" \
"v_mov_b32 v7, 0\n" \
"v_mov_b32 v8, 0\n" \
"v_mov_b32 v9, 0\n" \
"v_mov_b32 v10, 0\n" \
"v_mov_b32 v11, 0\n" \
"v_mov_b32 v12, 0\n" \
"v_mov_b32 v13, 0\n" \
"v_mov_b32 v14, 0\n" \
"v_mov_b32 v15, 0\n" \
"s_add_u32 s42, s63, s45\n" \
"v_mov_b32 v28, s42\n" \
"s_add_u32 s42, s62, 1\n" \
"s_bitcmp1_b32 s42, 1\n" \
"s_cselect_b64 exec, 255, 0\n" \
"global_store_dwordx4 v114, v[28:31], s[38:39] sc1\n" \
"s_mov_b64 exec, -1\n" \
"s_waitcnt lgkmcnt(0)\n" \
"s_barrier\n" \
"ds_read_b128 v[96:99], v114 offset:29184\n" \
"ds_read_b128 v[100:103], v114 offset:30208\n" \
"s_waitcnt lgkmcnt(1)\n" \
"v_smfmac_f32_16x16x64_bf16 v[0:3], v[96:99], v[192:199], v112\n" \
"v_smfmac_f32_16x16x64_bf16 v[4:7], v[96:99], v[200:207], v112\n" \
"s_waitcnt lgkmcnt(0)\n" \
"v_smfmac_f32_16x16x64_bf16 v[0:3], v[100:103], v[208:215], v112\n" \
"v_smfmac_f32_16x16x64_bf16 v[4:7], v[100:103], v[216:223], v112\n" \
"v_smfmac_f32_16x16x64_bf16 v[8:11], v[96:99], v[224:231], v112\n" \
"v_smfmac_f32_16x16x64_bf16 v[8:11], v[100:103], v[240:247], v112\n" \
"v_add_f32 v0, v0, v2\n" \
"v_add_f32 v1, v1, v3\n" \
"v_smfmac_f32_16x16x64_bf16 v[12:15], v[96:99], v[232:239], v112\n" \
"v_add_f32 v4, v4, v6\n" \
"v_add_f32 v5, v5, v7\n" \
"v_smfmac_f32_16x16x64_bf16 v[12:15], v[100:103], v[248:255], v112\n" \
"v_cvt_pk_bf16_f32 v32, v0, v4\n" \
"v_cvt_pk_bf16_f32 v34, v1, v5\n" \
"v_add_f32 v8, v8, v10\n" \
"v_add_f32 v9, v9, v11\n" \
"s_nop 3\n" \
"v_add_f32 v12, v12, v14\n" \
"v_add_f32 v13, v13, v15\n" \
"v_cvt_pk_bf16_f32 v33, v8, v12\n" \
"v_cvt_pk_bf16_f32 v35, v9, v13\n" \
"global_store_dwordx4 v124, v[32:35], s[48:49] sc1\n" \
"s_cmp_lg_u32 s62, 0\n" \
"s_cbranch_scc1 Lw1%=\n" \
"Lrd0%=:\n" \
"v_cmp_ne_u32 vcc, s45, v93\n" \
"s_cbranch_vccnz Lstale%=\n" \
"v_subrev_u32 v52, s70, v92\n" \
"v_min_i32 v52, 150, v52\n" \
"v_mov_b32 v8, -1\n" \
"v_mov_b32 v9, -1\n" \
"v_max_i32_dpp v52, v52, v52 row_ror:8 row_mask:0xf bank_mask:0xf\n" \
"v_mov_b32 v10, -1\n" \
"v_mov_b32 v11, -1\n" \
"v_max_i32_dpp v52, v52, v52 row_ror:4 row_mask:0xf bank_mask:0xf\n" \
"v_mov_b32 v12, -1\n" \
"v_mov_b32 v13, -1\n" \
"v_max_i32_dpp v52, v52, v52 row_ror:2 row_mask:0xf bank_mask:0xf\n" \
"v_mov_b32 v14, -1\n" \
"v_mov_b32 v15, -1\n" \
"v_max_i32_dpp v52, v52, v52 row_ror:1 row_mask:0xf bank_mask:0xf\n" \
"v_mov_b32 v16, -1\n" \
"v_mov_b32 v53, v52\n" \
"v_mov_b32 v17, -1\n" \
"v_mov_b32 v18, -1\n" \
"v_permlane16_swap_b32 v52, v53\n" \
"v_max_i32 v52, v52, v53\n" \
"v_mov_b32 v19, -1\n" \
"v_mov_b32 v20, -1\n" \
"v_readlane_b32 s42, v52, 0\n" \
"v_readlane_b32 s43, v52, 32\n" \
"v_mov_b32 v21, -1\n" \
"v_mov_b32 v22, -1\n" \
"v_mov_b32 v23, -1\n" \
"s_max_i32 s43, s43, s42\n" \
"s_add_u32 s43, s43, s70\n" \
"s_add_u32 s43, s43, 8\n" \
"Lu1%=:\n" \
"s_memrealtime s[74:75]\n" \
"s_waitcnt lgkmcnt(0)\n" \
"s_sub_u32 s42, s74, s43\n" \
"s_cmp_lt_i32 s42, 0\n" \
"s_cbranch_scc1 Lu1%=\n" \
"global_load_dwordx4 v[8:11], v114, s[50:51] offset:0 sc1\n" \
"global_load_dwordx4 v[12:15], v114, s[50:51] offset:1024 sc1\n" \
"global_load_dwordx4 v[16:19], v114, s[50:51] offset:2048 sc1\n" \
"global_load_dwordx4 v[20:23], v114, s[50:51] offset:3072 sc1\n" \
"v_mov_b32 v0, -1\n" \
"v_mov_b32 v1, -1\n" \
"v_mov_b32 v2, -1\n" \
"v_mov_b32 v3, -1\n" \
"v_mov_b32 v4, -1\n" \
"v_mov_b32 v5, -1\n" \
"v_mov_b32 v6, -1\n" \
"v_mov_b32 v7, -1\n" \
"s_add_u32 s43, s43, 48\n" \
"Lw0a%=:\n" \
"ds_read_b128 v[56:59], v113 offset:0\n" \
"ds_read_b128 v[60:63], v113 offset:1024\n" \
"ds_read_b128 v[64:67], v113 offset:2048\n" \
"ds_read_b128 v[68:71], v113 offset:3072\n" \
"ds_read_b128 v[72:75], v113 offset:4096\n" \
"ds_read_b128 v[76:79], v113 offset:5120\n" \
"ds_read_b128 v[80:83], v113 offset:6144\n" \
"ds_read_b128 v[84:87], v113 offset:7168\n" \
"ds_read_b128 v[88:91], v113 offset:8192\n" \
"ds_read_b128 v[96:99], v113 offset:10240\n" \
"ds_read_b128 v[100:103], v113 offset:11264\n" \
"v_mov_b32 v24, -1\n" \
"v_mov_b32 v25, -1\n" \
"v_mov_b32 v26, -1\n" \
"v_mov_b32 v27, -1\n" \
"s_add_u32 s47, s47, 0x8000\n" \
"s_cmp_ge_u32 s47, 163840\n" \
"s_cselect_b32 s42, 0x20000, 0\n" \
"s_sub_u32 s47, s47, s42\n" \
"s_add_u32 s56, s56, 524288\n" \
"s_addc_u32 s57, s57, 0\n" \
"s_add_u32 s35, s35, 0x8000\n" \
"s_cmp_ge_u32 s35, 163840\n" \
"s_cselect_b32 s42, 0x20000, 0\n" \
"s_sub_u32 s35, s35, s42\n" \
"s_mov_b32 m0, s35\n" \
"s_xor_b32 s48, s48, 0x100000\n" \
"s_add_u32 s45, s45, 1\n" \
"s_add_u32 s42, s62, 1\n" \
"s_bitcmp1_b32 s42, 1\n" \
"s_cbranch_scc0 Lnokm%=\n" \
"v_add_u32 v29, s64, v121\n" \
"v_add_u32 v30, s65, v119\n" \
"ds_read_b64 v[16:17], v29\n" \
"ds_read2_b32 v[18:19], v30 offset0:0 offset1:64\n" \
"Lnokm%=:\n" \
"s_add_u32 s42, s45, 2\n" \
"s_min_u32 s42, s42, 63\n" \
"s_cmp_lt_u32 s42, s67\n" \
"s_cbranch_scc0 Lkvw%=\n" \
"Lkvok%=:\n" \
"s_cmp_lg_u32 s62, 0\n" \
"s_cbranch_scc1 Lnored1%=\n" \
"Lred%=:\n" \
"v_max3_u32 v44, v8, v9, v10\n" \
"v_max_u32 v44, v44, v11\n" \
"v_mfma_f32_16x16x32_bf16 v[40:43], v[108:111], v[8:11], 0\n" \
"v_max3_u32 v44, v44, v12, v13\n" \
"v_max3_u32 v44, v44, v14, v15\n" \
"v_mfma_f32_16x16x32_bf16 v[40:43], v[108:111], v[12:15], v[40:43]\n" \
"v_max3_u32 v44, v44, v16, v17\n" \
"v_max3_u32 v44, v44, v18, v19\n" \
"v_mfma_f32_16x16x32_bf16 v[40:43], v[108:111], v[16:19], v[40:43]\n" \
"v_max3_u32 v44, v44, v20, v21\n" \
"v_max3_u32 v44, v44, v22, v23\n" \
"v_cmp_eq_u32 vcc, -1, v44\n" \
"v_mfma_f32_16x16x32_bf16 v[40:43], v[108:111], v[20:23], v[40:43]\n" \
"s_cbranch_vccz Lredok%=\n" \
"s_nop 1\n" \
"v_max3_u32 v44, v8, v9, v10\n" \
"v_max_u32 v44, v44, v11\n" \
"s_nop 0\n" \
"v_mfma_f32_16x16x32_bf16 v[40:43], v[108:111], v[8:11], 0\n" \
"v_max3_u32 v44, v44, v12, v13\n" \
"v_max3_u32 v44, v44, v14, v15\n" \
"v_mfma_f32_16x16x32_bf16 v[40:43], v[108:111], v[12:15], v[40:43]\n" \
"v_max3_u32 v44, v44, v16, v17\n" \
"v_max3_u32 v44, v44, v18, v19\n" \
"v_mfma_f32_16x16x32_bf16 v[40:43], v[108:111], v[16:19], v[40:43]\n" \
"v_max3_u32 v44, v44, v20, v21\n" \
"v_max3_u32 v44, v44, v22, v23\n" \
"v_cmp_eq_u32 vcc, -1, v44\n" \
"v_mfma_f32_16x16x32_bf16 v[40:43], v[108:111], v[20:23], v[40:43]\n" \
"s_cbranch_vccz Lredok%=\n" \
"s_nop 1\n" \
"v_max3_u32 v44, v8, v9, v10\n" \
"v_max_u32 v44, v44, v11\n" \
"s_nop 0\n" \
"v_mfma_f32_16x16x32_bf16 v[40:43], v[108:111], v[8:11], 0\n" \
"v_max3_u32 v44, v44, v12, v13\n" \
"v_max3_u32 v44, v44, v14, v15\n" \
"v_mfma_f32_16x16x32_bf16 v[40:43], v[108:111], v[12:15], v[40:43]\n" \
"v_max3_u32 v44, v44, v16, v17\n" \
"v_max3_u32 v44, v44, v18, v19\n" \
"v_mfma_f32_16x16x32_bf16 v[40:43], v[108:111], v[16:19], v[40:43]\n" \
"v_max3_u32 v44, v44, v20, v21\n" \
"v_max3_u32 v44, v44, v22, v23\n" \
"v_cmp_eq_u32 vcc, -1, v44\n" \
"v_mfma_f32_16x16x32_bf16 v[40:43], v[108:111], v[20:23], v[40:43]\n" \
"s_cbranch_vccz Lredok%=\n" \
"s_nop 1\n" \
"v_max3_u32 v44, v8, v9, v10\n" \
"v_max_u32 v44, v44, v11\n" \
"s_nop 0\n" \
"v_mfma_f32_16x16x32_bf16 v[40:43], v[108:111], v[8:11], 0\n" \
"v_max3_u32 v44, v44, v12, v13\n" \
"v_max3_u32 v44, v44, v14, v15\n" \
"v_mfma_f32_16x16x32_bf16 v[40:43], v[108:111], v[12:15], v[40:43]\n" \
"v_max3_u32 v44, v44, v16, v17\n" \
"v_max3_u32 v44, v44, v18, v19\n" \
"v_mfma_f32_16x16x32_bf16 v[40:43], v[108:111], v[16:19], v[40:43]\n" \
"v_max3_u32 v44, v44, v20, v21\n" \
"v_max3_u32 v44, v44, v22, v23\n" \
"v_cmp_eq_u32 vcc, -1, v44\n" \
"v_mfma_f32_16x16x32_bf16 v[40:43], v[108:111], v[20:23], v[40:43]\n" \
"s_cbranch_vccz Lredok%=\n" \
"s_nop 1\n" \
"v_max3_u32 v44, v8, v9, v10\n" \
"v_max_u32 v44, v44, v11\n" \
"s_nop 0\n" \
"v_mfma_f32_16x16x32_bf16 v[40:43], v[108:111], v[8:11], 0\n" \
"v_max3_u32 v44, v44, v12, v13\n" \
"v_max3_u32 v44, v44, v14, v15\n" \
"v_mfma_f32_16x16x32_bf16 v[40:43], v[108:111], v[12:15], v[40:43]\n" \
"v_max3_u32 v44, v44, v16, v17\n" \
"v_max3_u32 v44, v44, v18, v19\n" \
"v_mfma_f32_16x16x32_bf16 v[40:43], v[108:111], v[16:19], v[40:43]\n" \
"v_max3_u32 v44, v44, v20, v21\n" \
"v_max3_u32 v44, v44, v22, v23\n" \
"v_cmp_eq_u32 vcc, -1, v44\n" \
"v_mfma_f32_16x16x32_bf16 v[40:43], v[108:111], v[20:23], v[40:43]\n" \
"s_cbranch_vccz Lredok%=\n" \
"s_nop 1\n" \
"global_load_dwordx4 v[8:11], v114, s[50:51] offset:0 sc1\n" \
"global_load_dwordx4 v[12:15], v114, s[50:51] offset:1024 sc1\n" \
"global_load_dwordx4 v[16:19], v114, s[50:51] offset:2048 sc1\n" \
"global_load_dwordx4 v[20:23], v114, s[50:51] offset:3072 sc1\n" \
"s_sub_u32 s46, s46, 1\n" \
"s_cbranch_scc0 Lred%=\n" \
"s_branch Lend%=\n" \
"Lredok%=:\n" \
"s_nop 6\n" \
"v_add_f32_dpp v40, v40, v40 row_ror:8 row_mask:0xf bank_mask:0xf\n" \
"v_add_f32_dpp v41, v41, v41 row_ror:8 row_mask:0xf bank_mask:0xf\n" \
"v_add_f32_dpp v42, v42, v42 row_ror:8 row_mask:0xf bank_mask:0xf\n" \
"v_add_f32_dpp v43, v43, v43 row_ror:8 row_mask:0xf bank_mask:0xf\n" \
"v_cvt_pk_bf16_f32 v40, v40, v41\n" \
"v_cvt_pk_bf16_f32 v41, v42, v43\n" \
"s_nop 0\n" \
"v_mov_b32_dpp v42, v40 row_ror:4 row_mask:0xf bank_mask:0xf\n" \
"v_mov_b32_dpp v43, v41 row_ror:4 row_mask:0xf bank_mask:0xf\n" \
"s_mov_b64 exec, 0x000f000f\n" \
"global_store_dwordx4 v126, v[40:43], s[52:53] sc1\n" \
"s_mov_b64 exec, -1\n" \
"ds_read_b128 v[92:95], v113 offset:9216\n" \
"v_mov_b32 v32, 0\n" \
"v_mov_b32 v33, 0\n" \
"v_mov_b32 v34, 0\n" \
"v_mov_b32 v35, 0\n" \
"v_mov_b32 v36, 0\n" \
"v_mov_b32 v37, 0\n" \
"v_mov_b32 v38, 0\n" \
"v_mov_b32 v39, 0\n" \
"v_mov_b32 v40, 0\n" \
"v_mov_b32 v41, 0\n" \
"v_mov_b32 v42, 0\n" \
"v_mov_b32 v43, 0\n" \
"v_mov_b32 v44, 0\n" \
"v_mov_b32 v45, 0\n" \
"v_mov_b32 v46, 0\n" \
"v_mov_b32 v47, 0\n" \
"v_mov_b32 v48, 0\n" \
"v_mov_b32 v49, 0\n" \
"v_mov_b32 v50, 0\n" \
"v_mov_b32 v51, 0\n" \
"v_mov_b32 v52, 0\n" \
"v_mov_b32 v53, 0\n" \
"v_mov_b32 v54, 0\n" \
"v_mov_b32 v55, 0\n" \
"v_add_u32 v127, s47, v114\n" \
"Lu2%=:\n" \
"s_memrealtime s[74:75]\n" \
"s_waitcnt lgkmcnt(0)\n" \
"s_sub_u32 s42, s74, s43\n" \
"s_cmp_lt_i32 s42, 0\n" \
"s_cbranch_scc1 Lu2%=\n" \
"Lnored%=:\n" \
"s_barrier\n" \
"global_load_dwordx4 v[0:3], v125, s[52:53] offset:0 sc1\n" \
"global_load_dwordx4 v[4:7], v125, s[52:53] offset:1024 sc1\n" \
"s_xor_b32 s50, s50, 0x100000\n" \
"s_waitcnt lgkmcnt(0)\n" \
"v_max3_u32 v28, v0, v1, v2\n" \
"v_max3_u32 v28, v28, v3, v4\n" \
"v_max3_u32 v28, v28, v5, v6\n" \
"v_max_u32 v28, v28, v7\n" \
"v_cmp_eq_u32 vcc, -1, v28\n" \
"s_cbranch_vccz Lstep%=\n" \
"v_max3_u32 v28, v0, v1, v2\n" \
"v_max3_u32 v28, v28, v3, v4\n" \
"v_max3_u32 v28, v28, v5, v6\n" \
"v_max_u32 v28, v28, v7\n" \
"v_cmp_eq_u32 vcc, -1, v28\n" \
"s_cbranch_vccz Lstep%=\n" \
"v_max3_u32 v28, v0, v1, v2\n" \
"v_max3_u32 v28, v28, v3, v4\n" \
"v_max3_u32 v28, v28, v5, v6\n" \
"v_max_u32 v28, v28, v7\n" \
"v_cmp_eq_u32 vcc, -1, v28\n" \
"s_cbranch_vccz Lstep%=\n" \
"v_max3_u32 v28, v0, v1, v2\n" \
"v_max3_u32 v28, v28, v3, v4\n" \
"v_max3_u32 v28, v28, v5, v6\n" \
"v_max_u32 v28, v28, v7\n" \
"v_cmp_eq_u32 vcc, -1, v28\n" \
"s_cbranch_vccz Lstep%=\n" \
"v_max3_u32 v28, v0, v1, v2\n" \
"v_max3_u32 v28, v28, v3, v4\n" \
"v_max3_u32 v28, v28, v5, v6\n" \
"v_max_u32 v28, v28, v7\n" \
"v_cmp_eq_u32 vcc, -1, v28\n" \
"s_cbranch_vccz Lstep%=\n" \
"v_max3_u32 v28, v0, v1, v2\n" \
"v_max3_u32 v28, v28, v3, v4\n" \
"v_max3_u32 v28, v28, v5, v6\n" \
"v_max_u32 v28, v28, v7\n" \
"v_cmp_eq_u32 vcc, -1, v28\n" \
"s_cbranch_vccz Lstep%=\n" \
"v_max3_u32 v28, v0, v1, v2\n" \
"v_max3_u32 v28, v28, v3, v4\n" \
"v_max3_u32 v28, v28, v5, v6\n" \
"v_max_u32 v28, v28, v7\n" \
"v_cmp_eq_u32 vcc, -1, v28\n" \
"s_cbranch_vccz Lstep%=\n" \
"v_max3_u32 v28, v0, v1, v2\n" \
"v_max3_u32 v28, v28, v3, v4\n" \
"v_max3_u32 v28, v28, v5, v6\n" \
"v_max_u32 v28, v28, v7\n" \
"v_cmp_eq_u32 vcc, -1, v28\n" \
"s_cbranch_vccz Lstep%=\n" \
"Lpoll%=:\n" \
"v_max3_u32 v28, v0, v1, v2\n" \
"v_max3_u32 v28, v28, v3, v4\n" \
"v_max3_u32 v28, v28, v5, v6\n" \
"v_max_u32 v28, v28, v7\n" \
"v_cmp_eq_u32 vcc, -1, v28\n" \
"s_cbranch_vccz Lstep%=\n" \
"v_max3_u32 v28, v0, v1, v2\n" \
"v_max3_u32 v28, v28, v3, v4\n" \
"v_max3_u32 v28, v28, v5, v6\n" \
"v_max_u32 v28, v28, v7\n" \
"v_cmp_eq_u32 vcc, -1, v28\n" \
"s_cbranch_vccz Lstep%=\n" \
"v_max3_u32 v28, v0, v1, v2\n" \
"v_max3_u32 v28, v28, v3, v4\n" \
"v_max3_u32 v28, v28, v5, v6\n" \
"v_max_u32 v28, v28, v7\n" \
"v_cmp_eq_u32 vcc, -1, v28\n" \
"s_cbranch_vccz Lstep%=\n" \
"v_max3_u32 v28, v0, v1, v2\n" \
"v_max3_u32 v28, v28, v3, v4\n" \
"v_max3_u32 v28, v28, v5, v6\n" \
"v_max_u32 v28, v28, v7\n" \
"v_cmp_eq_u32 vcc, -1, v28\n" \
"s_cbranch_vccz Lstep%=\n" \
"global_load_dwordx4 v[0:3], v125, s[52:53] offset:0 sc1\n" \
"global_load_dwordx4 v[4:7], v125, s[52:53] offset:1024 sc1\n" \
"s_sub_u32 s46, s46, 1\n" \
"s_cbranch_scc0 Lpoll%=\n" \
"s_branch Lend%=\n" \
"Lnored1%=:\n" \
"ds_read_b128 v[92:95], v113 offset:9216\n" \
"v_mov_b32 v32, 0\n" \
"v_mov_b32 v33, 0\n" \
"v_mov_b32 v34, 0\n" \
"v_mov_b32 v35, 0\n" \
"v_mov_b32 v36, 0\n" \
"v_mov_b32 v37, 0\n" \
"v_mov_b32 v38, 0\n" \
"v_mov_b32 v39, 0\n" \
"v_mov_b32 v40, 0\n" \
"v_mov_b32 v41, 0\n" \
"v_mov_b32 v42, 0\n" \
"v_mov_b32 v43, 0\n" \
"v_mov_b32 v44, 0\n" \
"v_mov_b32 v45, 0\n" \
"v_mov_b32 v46, 0\n" \
"v_mov_b32 v47, 0\n" \
"v_mov_b32 v48, 0\n" \
"v_mov_b32 v49, 0\n" \
"v_mov_b32 v50, 0\n" \
"v_mov_b32 v51, 0\n" \
"v_mov_b32 v52, 0\n" \
"v_mov_b32 v53, 0\n" \
"v_mov_b32 v54, 0\n" \
"v_mov_b32 v55, 0\n" \
"v_add_u32 v127, s47, v114\n" \
"s_cmp_lt_u32 s45, 61\n" \
"s_cbranch_scc1 Lnored%=\n" \
"s_waitcnt lgkmcnt(0)\n" \
"s_add_u32 s42, s62, 1\n" \
"s_bitcmp1_b32 s42, 1\n" \
"s_cselect_b64 exec, -1, 0\n" \
"global_store_dwordx4 v114, v[16:19], s[54:55] sc1\n" \
"s_mov_b64 exec, -1\n" \
"s_waitcnt vmcnt(0)\n" \
"s_add_u32 s42, s63, s45\n" \
"v_mov_b32 v28, s42\n" \
"s_add_u32 s42, s62, 1\n" \
"s_bitcmp1_b32 s42, 1\n" \
"s_cselect_b64 exec, 255, 0\n" \
"global_store_dwordx4 v114, v[28:31], s[38:39] sc1\n" \
"s_mov_b64 exec, -1\n" \
"s_branch Lnored%=\n" \
"Lw1%=:\n" \
"s_memrealtime s[74:75]\n" \
"v_mov_b32 v0, -1\n" \
"v_mov_b32 v1, -1\n" \
"v_mov_b32 v2, -1\n" \
"v_mov_b32 v3, -1\n" \
"v_mov_b32 v4, -1\n" \
"v_mov_b32 v5, -1\n" \
"v_mov_b32 v6, -1\n" \
"v_mov_b32 v7, -1\n" \
"s_waitcnt lgkmcnt(0)\n" \
"s_sub_u32 s42, s74, s70\n" \
"s_mul_i32 s66, s66, 3\n" \
"s_lshr_b32 s66, s66, 2\n" \
"s_min_u32 s42, s42, 120\n" \
"s_add_u32 s66, s66, s42\n" \
"s_branch Lw0a%=\n" \
"Lkvw%=:\n" \
"v_mov_b32 v29, 0\n" \
"global_load_dwordx2 v[30:31], v29, s[36:37] sc1\n" \
"s_waitcnt vmcnt(0)\n" \
"v_readfirstlane_b32 s74, v30\n" \
"v_readfirstlane_b32 s75, v31\n" \
"s_sub_u32 s74, s74, s63\n" \
"s_cmp_gt_u32 s74, 64\n" \
"s_cselect_b32 s74, 0, s74\n" \
"s_sub_u32 s75, s75, s63\n" \
"s_cmp_gt_u32 s75, 64\n" \
"s_cselect_b32 s75, 0, s75\n" \
"s_lshl_b32 s74, s74, 1\n" \
"s_lshl1_add_u32 s75, s75, 1\n" \
"s_min_u32 s67, s74, s75\n" \
"s_cmp_lt_u32 s42, s67\n" \
"s_cbranch_scc1 Lkvok%=\n" \
"s_sub_u32 s46, s46, 1\n" \
"s_cbranch_scc1 Lend%=\n" \
"s_sleep 2\n" \
"s_branch Lkvw%=\n" \
"Lstale%=:\n" \
"global_load_dwordx2 v[92:93], v94, s[58:59] sc1\n" \
"s_sub_u32 s46, s46, 1\n" \
"s_cbranch_scc1 Lend%=\n" \
"s_waitcnt vmcnt(0)\n" \
"s_branch Lrd0%=\n" \
"Lfin%=:\n" \
"s_waitcnt lgkmcnt(0)\n" \
"s_barrier\n" \
"s_add_u32 s42, s62, 1\n" \
"s_bitcmp1_b32 s42, 1\n" \
"s_cbranch_scc0 Lnokm2%=\n" \
"v_add_u32 v29, s64, v121\n" \
"v_add_u32 v30, s65, v119\n" \
"ds_read_b64 v[16:17], v29\n" \
"ds_read2_b32 v[18:19], v30 offset0:0 offset1:64\n" \
"Lnokm2%=:\n" \
"s_waitcnt lgkmcnt(0)\n" \
"s_add_u32 s42, s62, 1\n" \
"s_bitcmp1_b32 s42, 1\n" \
"s_cselect_b64 exec, -1, 0\n" \
"global_store_dwordx4 v114, v[16:19], s[54:55] sc1\n" \
"s_mov_b64 exec, -1\n" \
"s_add_u32 s54, s54, 131072\n" \
"s_addc_u32 s55, s55, 0\n" \
"s_add_u32 s42, s63, 64\n" \
"v_mov_b32 v28, s42\n" \
"s_add_u32 s42, s62, 1\n" \
"s_bitcmp1_b32 s42, 1\n" \
"s_waitcnt vmcnt(0)\n" \
"s_cselect_b64 exec, 255, 0\n" \
"global_store_dwordx4 v114, v[28:31], s[38:39] sc1\n" \
"s_mov_b64 exec, -1\n" \
"Lend%=:\n" \
"s_waitcnt vmcnt(0)\n" \
"s_waitcnt lgkmcnt(0)\n"
#define CHAIN_CLOB "v0","v1","v2","v3","v4","v5","v6","v7","v8","v9","v10","v11","v12","v13","v14","v15","v16","v17","v18","v19","v20","v21","v22","v23","v24","v25","v26","v27","v28","v29","v30","v31","v32","v33","v34","v35","v36","v37","v38","v39","v40","v41","v42","v43","v44","v45","v46","v47","v48","v49","v50","v51","v52","v53","v54","v55","v56","v57","v58","v59","v60","v61","v62","v63","v64","v65","v66","v67","v68","v69","v70","v71","v72","v73","v74","v75","v76","v77","v78","v79","v80","v81","v82","v83","v84","v85","v86","v87","v88","v89","v90","v91","v92","v93","v94","v95","v96","v97","v98","v99","v100","v101","v102","v103","v104","v105","v106","v107","v108","v109","v110","v111","v112","v113","v114","v115","v116","v117","v118","v119","v120","v121","v122","v123","v124","v125","v126","v127","v128","v129","v130","v131","v132","v133","v134","v135","v136","v137","v138","v139","v140","v141","v142","v143","v144","v145","v146","v147","v148","v149","v150","v151","v152","v153","v154","v155","v156","v157","v158","v159","v160","v161","v162","v163","v164","v165","v166","v167","v168","v169","v170","v171","v172","v173","v174","v175","v176","v177","v178","v179","v180","v181","v182","v183","v184","v185","v186","v187","v188","v189","v190","v191","v192","v193","v194","v195","v196","v197","v198","v199","v200","v201","v202","v203","v204","v205","v206","v207","v208","v209","v210","v211","v212","v213","v214","v215","v216","v217","v218","v219","v220","v221","v222","v223","v224","v225","v226","v227","v228","v229","v230","v231","v232","v233","v234","v235","v236","v237","v238","v239","v240","v241","v242","v243","v244","v245","v246","v247","v248","v249","v250","v251","v252","v253","v254","v255","a0","a1","a2","a3","a4","a5","a6","a7","a8","a9","a10","a11","a12","a13","a14","a15","a16","a17","a18","a19","a20","a21","a22","a23","a24","a25","a26","a27","a28","a29","a30","a31","a32","a33","a34","a35","a36","a37","a38","a39","a40","a41","a42","a43","a44","a45","a46","a47","a48","a49","a50","a51","a52","a53","a54","a55","a56","a57","a58","a59","a60","a61","a62","a63","a64","a65","a66","a67","a68","a69","a70","a71","a72","a73","a74","a75","a76","a77","a78","a79","a80","a81","a82","a83","a84","a85","a86","a87","a88","a89","a90","a91","a92","a93","a94","a95","a96","a97","a98","a99","a100","a101","a102","a103","a104","a105","a106","a107","a108","a109","a110","a111","a112","a113","a114","a115","a116","a117","a118","a119","a120","a121","a122","a123","a124","a125","a126","a127","a128","a129","a130","a131","a132","a133","a134","a135","a136","a137","a138","a139","a140","a141","a142","a143","a144","a145","a146","a147","a148","a149","a150","a151","a152","a153","a154","a155","a156","a157","a158","a159","a160","a161","a162","a163","a164","a165","a166","a167","a168","a169","a170","a171","a172","a173","a174","a175","a176","a177","a178","a179","a180","a181","a182","a183","a184","a185","a186","a187","a188","a189","a190","a191","a192","a193","a194","a195","a196","a197","a198","a199","a200","a201","a202","a203","a204","a205","a206","a207","a208","a209","a210","a211","a212","a213","a214","a215","a216","a217","a218","a219","a220","a221","a222","a223","a224","a225","a226","a227","a228","a229","a230","a231","a232","a233","a234","a235","a236","a237","a238","a239","a240","a241","a242","a243","a244","a245","a246","a247","a248","a249","a250","a251","a252","a253","a254","a255","s20","s21","s22","s23","s24","s25","s26","s27","s28","s29","s30","s31","s34","s35","s36","s37","s38","s39","s40","s41","s42","s43","s44","s45","s46","s47","s48","s49","s50","s51","s52","s53","s54","s55","s56","s57","s58","s59","s60","s61","s62","s63","s64","s65","s66","s67","s68","s69","s70","s71","s72","s73","s74","s75","s76","s77","s78","s79","s80","s81","s82","s83","s84","s85","s86","s87","s88","s89","s90","s91","s92","s93","s94","s95","s96","s97","s98","s99","vcc","scc","memory"

#define CH5 1
#define DI __device__ __forceinline__
typedef unsigned u32; typedef unsigned long long u64; typedef unsigned short u16;
typedef u32 u32x4 __attribute__((ext_vector_type(4)));
typedef u32 u32x2 __attribute__((ext_vector_type(2)));
typedef float f32x4 __attribute__((ext_vector_type(4)));
typedef float f32x2 __attribute__((ext_vector_type(2)));
typedef __bf16 bf16x8 __attribute__((ext_vector_type(8)));
typedef __bf16 bf16x2 __attribute__((ext_vector_type(2)));
typedef short s16x4 __attribute__((ext_vector_type(4)));
typedef __attribute__((address_space(3))) char* ldsp;
typedef __amdgpu_buffer_rsrc_t rsrc;

constexpr u32 MB=1u<<20;
constexpr u32 O_KMV=5*MB,O_KV=16*MB,O_Q=50*MB,O_H=66*MB,O_PART=68*MB,O_X1=132*MB,O_U=13*MB,O_H2=136*MB,O_Z1=138*MB,O_Z2=141*MB,O_ST=144*MB,O_FL=145*MB,O_DBG=146*MB,WS_USED=147*MB;
constexpr u32 F_EP=O_FL,F_SEL=O_FL+256,F_ROT=O_FL+512,F_HS=O_FL+0x1000,F_STA=O_FL+0x4000,F_MOD=O_FL+0x5000,F_H=O_FL+0x8000,F_PC=O_FL+0x9000,F_KM=O_FL+0x10000,F_UF=O_FL+0x20000,F_PF=O_FL+0x14000,F_ZF=F_PF+4096,F_RF=O_FL+0x18000,F_PH=O_FL+0x30000,F_AG=O_FL+0x38000;
#define NC_ 1
#define DW_ 1
#define SLW 4
#define FS 60
#define FZ 57
#define GT 2
#define GZ 3
#define SLZ 1
#define AD 2
#define PR 1
#define HN_ 0
constexpr int TMO=40000;
#define AB 0
#define SLP 4
#define SLA 2
struct Args{ const char *x,*mem0,*Wq,*Wk,*Wv,*Wo,*win,*wsp,*bsp,*wout,*c,*wmod; char *out,*ws0; };
#define WSP (A.ws0+((0u-(u32)(uintptr_t)A.ws0)&(4*MB-1)))

#define SB __builtin_amdgcn_sched_barrier(0)
DI void cbar(){ asm volatile("":::"memory"); }
DI void bar(){ asm volatile("s_waitcnt lgkmcnt(0)\ns_barrier":::"memory"); }
template<int N> DI void vmw(){ asm volatile("s_waitcnt vmcnt(%0)"::"n"(N):"memory"); }
DI u32 rfl(u32 v){ return __builtin_amdgcn_readfirstlane(v); }
DI u64 uni(const char* p){ u64 b=(u64)p; return (u64)rfl((u32)b)|((u64)rfl((u32)(b>>32))<<32); }
DI u32x4 ld16(rsrc r,u32 off){ return __builtin_amdgcn_raw_buffer_load_b128(r,off,0,16); }
DI u32x2 ld8(rsrc r,u32 off){ return __builtin_amdgcn_raw_buffer_load_b64(r,off,0,16); }
#define PV (r<64)
#define AT 0
#define ZA 16
#define HS " sc1"
DI u32x4 ld16n(rsrc r,u32 off){ return __builtin_amdgcn_raw_buffer_load_b128(r,off,0,ZA); }
DI u32x2 ld8n(rsrc r,u32 off){ return __builtin_amdgcn_raw_buffer_load_b64(r,off,0,ZA); }
DI u32 ld4(rsrc r,u32 off){ cbar(); u32 v=__builtin_amdgcn_raw_buffer_load_b32(r,off,0,16); cbar(); return v; }
DI void st16(rsrc r,u32 off,u32x4 v){ __builtin_amdgcn_raw_buffer_store_b128(v,r,off,0,16); }
DI void st8(rsrc r,u32 off,u32x2 v){ __builtin_amdgcn_raw_buffer_store_b64(v,r,off,0,16); }
DI void st4(rsrc r,u32 off,u32 v){ cbar(); __builtin_amdgcn_raw_buffer_store_b32(v,r,off,0,16); cbar(); }
DI void dma(u32 l,const char* base,u32 voff){ asm volatile("s_mov_b32 m0, %0\ns_nop 0\nglobal_load_lds_dwordx4 %1, %2"::"s"(rfl(l)),"v"(voff),"s"(uni(base)):"memory"); }
DI void dmac(u32 l,const char* base,u32 voff){ asm volatile("s_mov_b32 m0, %0\ns_nop 0\nglobal_load_lds_dwordx4 %1, %2 sc1"::"s"(rfl(l)),"v"(voff),"s"(uni(base)):"memory"); }
DI void dmaf(u32 l,const char* base,u32 voff){ asm volatile("s_mov_b32 m0, %0\ns_nop 0\nglobal_load_lds_dword %1, %2 sc1"::"s"(rfl(l)),"v"(voff),"s"(uni(base)):"memory"); }
DI void flag(rsrc r,u32 off,u32 v,int l){ cbar(); if(l<8) st16(r,off+l*16,u32x4{v,v,v,v}); cbar(); }
DI u32x4 lr16(ldsp L,u32 o){ return *(__attribute__((address_space(3))) u32x4*)(L+o); }
DI void lw16(ldsp L,u32 o,u32x4 v){ *(__attribute__((address_space(3))) u32x4*)(L+o)=v; }
DI void lw8(ldsp L,u32 o,u32x2 v){ *(__attribute__((address_space(3))) u32x2*)(L+o)=v; }
DI u32 lr4(ldsp L,u32 o){ return *(__attribute__((address_space(3))) u32*)(L+o); }
DI void lw4(ldsp L,u32 o,u32 v){ *(__attribute__((address_space(3))) u32*)(L+o)=v; }
DI void lw2(ldsp L,u32 o,u32 v){ *(__attribute__((address_space(3))) u16*)(L+o)=(u16)v; }
DI u32x2 tr(ldsp L,u32 o){ return __builtin_bit_cast(u32x2,__builtin_amdgcn_ds_read_tr16_b64_v4i16((__attribute__((address_space(3))) s16x4*)(L+o))); }
DI u32x4 tr2(ldsp L,u32 o,u32 d){ u32x2 a=tr(L,o),b=tr(L,o+d); return u32x4{a.x,a.y,b.x,b.y}; }
DI f32x4 mm(u32x4 a,u32x4 b,f32x4 c){ return __builtin_amdgcn_mfma_f32_16x16x32_bf16(__builtin_bit_cast(bf16x8,a),__builtin_bit_cast(bf16x8,b),c,0,0,0); }
DI f32x4 mm16(u32x2 a,u32x2 b,f32x4 c){ return __builtin_amdgcn_mfma_f32_16x16x16bf16_1k(__builtin_bit_cast(s16x4,a),__builtin_bit_cast(s16x4,b),c,0,0,0); }
DI u32 pk(float a,float b){ bf16x2 v={(__bf16)a,(__bf16)b}; return __builtin_bit_cast(u32,v); }
DI u32x4 pk8(f32x4 a,f32x4 b){ return u32x4{pk(a.x,a.y),pk(a.z,a.w),pk(b.x,b.y),pk(b.z,b.w)}; }
DI float lo(u32 v){ return __builtin_bit_cast(float,v<<16); }
DI float hi(u32 v){ return __builtin_bit_cast(float,v&0xffff0000u); }
DI void up8(u32x4 v,float* f){ for(int i=0;i<4;i++){ f[2*i]=lo(v[i]); f[2*i+1]=hi(v[i]); } }
template<int C> DI float dpp(float v){ return __builtin_bit_cast(float,__builtin_amdgcn_update_dpp(0,__builtin_bit_cast(int,v),C,0xf,0xf,true)); }
DI float x16(float v,bool mx){ u32 a=__builtin_bit_cast(u32,v); auto r=__builtin_amdgcn_permlane16_swap(a,a,false,false); float p=__builtin_bit_cast(float,(u32)r[0]),q=__builtin_bit_cast(float,(u32)r[1]); return mx?fmaxf(p,q):p+q; }
DI float x32(float v,bool mx){ u32 a=__builtin_bit_cast(u32,v); auto r=__builtin_amdgcn_permlane32_swap(a,a,false,false); float p=__builtin_bit_cast(float,(u32)r[0]),q=__builtin_bit_cast(float,(u32)r[1]); return mx?fmaxf(p,q):p+q; }
DI float rsum16(float v){ v+=dpp<0x128>(v); v+=dpp<0x124>(v); v+=dpp<0x122>(v); v+=dpp<0x121>(v); return v; }
DI float wsum(float v){ return x32(x16(rsum16(v),false),false); }
constexpr u32 STC=0x30100;
#define PZ u32x4{~0u,~0u,~0u,~0u}
DI u32 umx(u32x4 v,u32 m){ return max(max(max(v.x,v.y),m),max(v.z,v.w)); }
DI bool rdy(u32 v,u32 e,u32 need){ u32 d=v-e; return d>=need && d<=64; }
#define VD 1
DI void waitf(rsrc W,u32 off,bool act,u32 e,u32 need,int& tmo){
  while(true){ u32 v=ld4(W,off); if(__all(!act || rdy(v,e,need))) break; if(--tmo<0) break; __builtin_amdgcn_s_sleep(SLW); }
}
DI void ldmod(rsrc W,u32 col,u32 e,float* f,int& tmo){
  while(true){ cbar(); u32x4 a=ld16(W,F_MOD+col*4),b=ld16(W,F_MOD+col*4+16); cbar();
    up8(u32x4{a.x,a.z,b.x,b.z},f);
    if(__all(a.y==e&&a.w==e&&b.y==e&&b.w==e)) break; if(--tmo<0) break; __builtin_amdgcn_s_sleep(2); }
}

DI u32 inbox_off(int j,int sel){ return (j>>1)*32768+(((sel&3)^(j&2))<<13)+(j&1)*4096; }
DI u32 mslot_off(int s,int sel){ return (2u<<20)+s*32768+(((sel>>2)^((s>>3)&2))<<13); }
DI void chain_role(const Args& A,rsrc W,ldsp L,u32 e,int wg){
  const int tid=threadIdx.x,h=wg>>1,be=wg&1,w=rfl(tid>>6),l=tid&63,g=l>>4,c=l&15;
  const u32x4 ff={~0u,~0u,~0u,~0u};
  int tmo=TMO;
  if(wg<8&&w==0){
    const u32 off=(wg>>2?mslot_off(1,(wg&3)<<2):inbox_off(0,wg))+l*16; u32 mn=1023;
    for(int r=0;r<NPR;r++){ u64 t0=__builtin_amdgcn_s_memtime(); if(l<8) st16(W,off,ff); vmw<0>(); u32 d=(u32)(__builtin_amdgcn_s_memtime()-t0); mn=d<mn?d:mn; }
    if(l==0) st4(W,F_SEL+wg*4,e+(mn>>3));
  }
  cbar();
  const int m=2*(c>>2)+(c&1), hf=(c>>1)&1, qq=c>>2, p=c&3;
  const u16* mem0=(const u16*)A.mem0;
  u32 mv_[2][4][2];
  for(int j=0;j<2;j++) for(int i=0;i<4;i++){ int col=64*(2*w+j)+32*(i&1)+8*(g>>1)+4*(i>>1)+2*(g&1)+hf; mv_[j][i][0]=mem0[m*512+col]; mv_[j][i][1]=mem0[m*512+col+16]; }
  cbar();
  for(int j=0;j<2;j++) lw16(L,(j*256+tid)*16,u32x4{mv_[j][0][0]|(mv_[j][0][1]<<16),mv_[j][1][0]|(mv_[j][1][1]<<16),mv_[j][2][0]|(mv_[j][2][1]<<16),mv_[j][3][0]|(mv_[j][3][1]<<16)});
  u32 sv; while(true){ sv=ld4(W,F_SEL+(l&7)*4)-e; if(__all(sv<128)||--tmo<0) break; __builtin_amdgcn_s_sleep(1); }
  { u32 q[8]; for(int i=0;i<8;i++) q[i]=__builtin_amdgcn_readlane(sv,i)*4+(i&3);
    sv=(min(min(q[0],q[1]),min(q[2],q[3]))&3)+4*(min(min(q[4],q[5]),min(q[6],q[7]))&3); }
  const int sel=sv&15;

  const u32 own=inbox_off(wg,sel);
  for(int i=0;i<4;i++) st16(W,O_KMV+max(56+2*i+(tid>>7),FS)*131072+wg*2048+(tid&127)*16,ff);
  for(int i=0;i<4;i+=2-AB) st16(W,(own^((i&1)<<14))+(i>>1)*0x100000+tid*16,ff);
  for(int i=0;i<4;i+=2-AB){ int s=(i>>1)*32+(tid>>3); st16(W,(mslot_off(s,sel)^((i&1)<<14))+wg*128+(tid&7)*16,ff); }
  { u32 v[4]; for(int i=0;i<4;i++) v[i]=(c<8 && (c>>1)==i)? (0x3f80u<<(16*(c&1))):0u;
    lw16(L,(2*256+tid)*16,u32x4{v[0],v[1],v[2],v[3]}); }
  auto base=[](int x){return x*512+32*(x&1)+128*(x>>1);};
  const int mp=c&7, tau=c>>3;
  lw16(L,(3*256+tid)*16,u32x4{hf?0xEEEEu:0x4444u,(u32)(0x8000+w*0x8000+l*16),(u32)(l*16),(u32)(0x4000+w*2048+l*16)});
  lw16(L,(4*256+tid)*16,u32x4{(u32)(0x4000+(w^1)*2048+l*16),(u32)(0x4000+(w^2)*2048+l*16),(u32)(0x4000+(w^3)*2048+l*16),(u32)(0x6000+(32*w+c)*16+4*g)});
  lw16(L,(5*256+tid)*16,u32x4{(u32)(0x6000+(32*(g>>1)+8*(g&1)+qq)*16+(p&1)*8),(u32)(0x6800+base(g)+(16*w+c)*8),(u32)(0x6800+base(p)+(8*g+qq)*8),
                     (u32)(0x7200+(w>>1)*1024+(16*(2*(w&1)+tau)+4*(mp>>1)+2*(g>>1)+(mp&1))*16+8*(g&1))});
  lw16(L,(6*256+tid)*16,u32x4{inbox_off(32*be+8*w+2*g+tau,sel)+h*128+mp*16,
   (u32)(w*2048+(m>>1)*256+(g>>1)*128+(m&1)*64+(2*(g&1)+hf)*16),(u32)(wg*128+(g&1)*64+p*16),0u});
  { int r=l>>2, a=r>>2, hh=(r>>1)&1, b=r&1;
    lw16(L,(7*256+tid)*16,u32x4{(u32)((32*(a&1)+16*b+4*(a>>1)+hh)*8192+(l&3)*16),(u32)((8*hh+2*a+b)*1024+(l&3)*16),
                       (u32)(0x8000+w*0x8000+(8*g+qq)*64+p*8),(u32)(l*128)}); }
  if(l==0){ auto S=(__attribute__((address_space(3))) u64*)(L+0x8000+w*0x8000); char* ws=WSP;
    S[0]=(u64)(A.Wq+h*256+w*64); S[1]=(u64)(A.Wk+h*256+w*64); S[2]=(u64)(A.Wv+h*256+w*64); S[3]=(u64)(A.Wo+h*131072+be*512+w*128);
    S[4]=(u64)ws; S[5]=(u64)(ws+(own^(w==3?0x4000:0))); S[6]=(u64)(ws+mslot_off(CH5?0:1,sel)); const int ck=2*be+w-1; S[7]=(u64)(ws+O_KMV-32*4096+h*4096+ck*1024); S[8]=(u64)(ws+O_KV+h*16384+w*4096);
    S[9]=(u64)(ws+F_HS); S[10]=(u64)(ws+O_DBG+(wg*4+w)*64); S[11]=(u64)w|((u64)e<<32); S[12]=(u64)(u32)((ck-w)*128)|((u64)(u32)((ck-w)*512)<<32); S[13]=(u64)(wg*128); S[14]=(u64)(ws+own+w*1024);
    S[15]=(u64)(ws+F_PC+((1+(w>>1))*32+h)*128); S[16]=(u64)(ws+F_KM+h*512+ck*128);
  }
  asm volatile("s_waitcnt lgkmcnt(0)":::"memory");
  asm volatile(CHAIN_ASM :: "s"(w) : CHAIN_CLOB);
}

DI void mod16(const Args& A,rsrc W,ldsp L,u32 e,int col0){
  const int tid=threadIdx.x,kg=tid>>1;
  u32x2 cv=*(const u32x2*)(A.c+kg*8); u32x4 wv[4];
  for(int i=0;i<4;i++) wv[i]=*(const u32x4*)(A.wmod+(size_t)(4*kg+i)*6144+col0*2+(tid&1)*16);
  float s[4]={lo(cv.x),hi(cv.x),lo(cv.y),hi(cv.y)},acc[8];
  for(int j=0;j<8;j++) acc[j]=0;
  for(int i=0;i<4;i++){ float t=(float)(__bf16)(s[i]/(1.f+__expf(-s[i]))),f[8]; up8(wv[i],f); for(int j=0;j<8;j++) acc[j]+=t*f[j]; }
  for(int j=0;j<8;j++){ float v=acc[j]; v+=dpp<0x128>(v); v+=dpp<0x124>(v); v+=dpp<0x122>(v); acc[j]=v; }
  if((tid&14)==0){ u32 o=(tid>>4)*64+(tid&1)*32; lw16(L,o,__builtin_bit_cast(u32x4,f32x4{acc[0],acc[1],acc[2],acc[3]})); lw16(L,o+16,__builtin_bit_cast(u32x4,f32x4{acc[4],acc[5],acc[6],acc[7]})); }
  bar();
  if(tid<4){ f32x4 t={0,0,0,0}; for(int i=0;i<16;i++) t+=__builtin_bit_cast(f32x4,lr16(L,i*64+tid*16));
    st16(W,F_MOD+(col0+tid*4)*4,u32x4{pk(t.x,t.y),e,pk(t.z,t.w),e}); }
  bar();
}
DI void dma_rows(u32 ldsb,const char* base,const u32* vo,int w){
  asm volatile("s_mov_b32 m0, %0\ns_nop 0\n"
    "global_load_lds_dwordx4 %2, %1" HS "\nglobal_load_lds_dwordx4 %3, %1 offset:1024" HS "\nglobal_load_lds_dwordx4 %4, %1 offset:2048" HS "\nglobal_load_lds_dwordx4 %5, %1 offset:3072" HS "\n"
    "s_add_u32 m0, m0, 3968\ns_nop 0\n"
    "global_load_lds_dwordx4 %2, %1 offset:128" HS "\nglobal_load_lds_dwordx4 %3, %1 offset:1152" HS "\nglobal_load_lds_dwordx4 %4, %1 offset:2176" HS "\nglobal_load_lds_dwordx4 %5, %1 offset:3200" HS
    ::"s"(rfl(ldsb+w*8192)),"s"(uni(base)),"v"(vo[0]),"v"(vo[1]),"v"(vo[2]),"v"(vo[3]):"memory","scc");
}
DI void dma_row1(u32 l,u64 base,const u32* vo,int n){
  asm volatile("s_add_u32 m0, %0, %3\ns_nop 0\nglobal_load_lds_dwordx4 %2, %1 offset:%4" HS::"s"(l),"s"(base),"v"(vo[n&3]),"n"((n>>2)*3968),"n"((n&3)*1024+(n>>2)*128):"scc");
}
DI void row_voff(u32* vo,int l,int w,bool perm){
  for(int q=0;q<4;q++){ int rho=8*q+(l>>3),c=rho&15,tok=perm?8*(c>>2)+4*(rho>>4)+(c&3):rho; vo[q]=(tok-q)*1024+w*256+(((l&7)^((rho>>1)&7))<<4); }
}
template<bool VT> DI void proj_loop(const Args& A,rsrc W,ldsp L,u32 e,int ty,int h,int par,int& tmo){
  const int tid=threadIdx.x,w=rfl(tid>>6),l=tid&63,g=l>>4,c=l&15,qq=c>>2,p=c&3;
  const char* wsrc=(ty==0?A.Wq:(ty==1?A.Wk:A.Wv))+h*256;
  #pragma unroll 4
  for(int n=0;n<32;n++){ int i=32*w+n,q=l>>4; u32 f=VT?((q<<1)|(((n>>1)&1)<<3)):(((q&1)<<1)|((q>>1)<<3));
    dma(i*1024,wsrc+(size_t)i*32768,q*8192+(((l&15)^f)<<4)); }
  vmw<0>(); bar();

  u32x4 Wf[2][16];
  for(int a=0;a<2;a++){ u32 ch=VT?4*w+2*a+(p>>1):8*(w&1)+2*(w>>1)+4*(p>>1)+(p&1), f=VT?((qq<<1)|((g&1)<<3)):(((qq&1)<<1)|((qq>>1)<<3));
    u32 o=(8*g+qq)*256+((ch^f)<<4)+(VT?(p&1)*8:a*8);
    for(int ks=0;ks<16;ks++) Wf[a][ks]=tr2(L,o+ks*8192,1024); }
  if(w==0) waitf(W,F_H+(par*32+(l&31))*4,true,e,1,tmo);
  bar();

  u32 vo[4]; row_voff(vo,l,w,false);
  const char* hb=WSP+O_H+par*32768;
  for(int i=0;i<3;i++) dma_rows(i*32768,hb+i*65536,vo,w);
  const u32 ro0=c*128+((g^(c>>1))<<4), ro1=c*128+(((4+g)^(c>>1))<<4);
  u32 oo=(ty==0?O_Q+(par*32+h)*8192:O_KV+(par*32+h)*16384)+(VT?8192+w*2048:w*1024)+l*16;
  const u32 ostep=ty==0?64*8192:64*16384, fo=F_PC+(ty*32+h)*128+par*4;

  #pragma unroll 1
  for(int it=0;it<32;it++){
    if(it==0) vmw<16>(); else if(it<3) vmw<0>(); else vmw<10>();

    bar();

    if(tid==0&&it>0) st4(W,fo,e+(it<3?it:it-1));
    const u32 dl=rfl(((it+3)&3)*32768+w*8192); const u64 db=uni(hb+min(it+3,31)*65536);
    f32x4 acc[2][2]={};
    const u32 sb=(it&3)*32768;
    u32x4 hf[16][2];
    #pragma unroll
    for(int ks=0;ks<4;ks++){ u32 o=sb+(ks>>1)*4096+((ks&1)?ro1:ro0); hf[ks][0]=lr16(L,o); hf[ks][1]=lr16(L,o+2048); }
    SB;
    #pragma unroll
    for(int ks=0;ks<16;ks++){
      if(ks<12){ u32 o=sb+((ks+4)>>1)*4096+((ks&1)?ro1:ro0); hf[ks+4][0]=lr16(L,o); hf[ks+4][1]=lr16(L,o+2048); }
      for(int a=0;a<2;a++){
        if(VT){ acc[0][a]=mm(hf[ks][0],Wf[a][ks],acc[0][a]); acc[1][a]=mm(hf[ks][1],Wf[a][ks],acc[1][a]); }
        else  { acc[0][a]=mm(Wf[a][ks],hf[ks][0],acc[0][a]); acc[1][a]=mm(Wf[a][ks],hf[ks][1],acc[1][a]); }
      }
      SB;
      if(!(ks&1)) dma_row1(dl,db,vo,ks>>1);
      SB;
    }
    if(VT){ st16(W,oo,pk8(acc[0][0],acc[1][0])); st16(W,oo+1024,pk8(acc[1][1],acc[0][1])); }
    else  { st16(W,oo,pk8(acc[0][0],acc[0][1])); st16(W,oo+4096,pk8(acc[1][0],acc[1][1])); }
    oo+=ostep;

  }

  vmw<0>(); bar();
  if(tid==0) st4(W,fo,e+32);

}
DI void ln1_role(const Args& A,rsrc W,ldsp L,u32 e,int j,int& tmo){
  const int tid=threadIdx.x,w=tid>>6,l=tid&63;
  u32x4 xv[8];
  for(int r=0;r<8;r++) xv[r]=*(const u32x4*)(A.x+(size_t)(j*32+w*8+r)*1024+l*16);
  mod16(A,W,L,e,16*j);

  float v[8][8];
  for(int r=0;r<8;r++){ up8(xv[r],v[r]); float s=0; for(int k=0;k<8;k++) s+=v[r][k]; float m=wsum(s)*(1.f/512);
    float q=0; for(int k=0;k<8;k++){ v[r][k]-=m; q+=v[r][k]*v[r][k]; } float rs=rsqrtf(wsum(q)*(1.f/512)+1e-5f);
    for(int k=0;k<8;k++) v[r][k]*=rs; }
  float sa[8],ca[8]; ldmod(W,8*l,e,sa,tmo); ldmod(W,512+8*l,e,ca,tmo);
  for(int r=0;r<8;r++){ u32x4 o; for(int k=0;k<4;k++) o[k]=pk(v[r][2*k]*(1.f+ca[2*k])+sa[2*k],v[r][2*k+1]*(1.f+ca[2*k+1])+sa[2*k+1]);
    st16(W,O_H+(j*32+w*8+r)*1024+l*16,o); }
  vmw<0>(); bar();
  if(tid==0) st4(W,F_H+((j&1)*32+(j>>1))*4,e+1);

  mod16(A,W,L,e,1024+32*j); mod16(A,W,L,e,1040+32*j);
}
DI void dma_qkv(u32 sl,const char* q,const char* kv,u32 vo,int w){
  asm volatile("s_mov_b32 m0, %0\ns_nop 0\nglobal_load_lds_dwordx4 %4, %2 sc1\nglobal_load_lds_dwordx4 %4, %2 offset:1024 sc1\ns_mov_b32 m0, %1\ns_nop 0\n"
    "global_load_lds_dwordx4 %4, %3 sc1\nglobal_load_lds_dwordx4 %4, %3 offset:1024 sc1\nglobal_load_lds_dwordx4 %4, %3 offset:2048 sc1\nglobal_load_lds_dwordx4 %4, %3 offset:3072 sc1"
    ::"s"(rfl(sl+w*2048)),"s"(rfl(sl+8192+w*4096)),"s"(uni(q+w*2048)),"s"(uni(kv+w*4096)),"v"(vo):"memory");
}
DI void attn_role(const Args& A,rsrc W,ldsp L,u32 e,int h,int a0,int a1,u32 pfo,int& tmo){
  const int tid=threadIdx.x,w=rfl(tid>>6),l=tid&63,g=l>>4,c=l&15,qq=c>>2,p=c&3,qt=w&1,eh=w>>1;
  #pragma unroll 4
  for(int n=0;n<32;n++){ int row=32*w+n; dma(row*1024,A.Wo+(size_t)(h*128+row)*1024,(l^((n&3)<<2))<<4); }
  vmw<0>(); bar();
  u32x4 Wf[8][4];
  for(int nt=0;nt<8;nt++){ u32 o=(4*g+qq)*1024+(((16*w+4*(nt>>1)+p)^(qq<<2))<<4)+(nt&1)*8;
    for(int kk=0;kk<4;kk++) Wf[nt][kk]=tr2(L,o+kk*32768,16384); }
  if(w==0) waitf(W,F_PC+((l>>1)*32+h)*128+(l&1)*4,l<6,e,32,tmo);
  bar();

  constexpr u32 LK=0,LO=0x2000,LR=0x4000,SL=24576;
  const u32 kfo=F_KM+h*512+w*128, to=(g>>1)*1024+(p*16+8*(g&1)+qq)*16;
  const char *qb=WSP+O_Q+h*8192,*kb=WSP+O_KV+h*16384,*mb=WSP+O_KMV+h*4096+w*1024;
  const float sc=0.08838834764f*1.44269504089f;
  dma_qkv(LR,qb+a0*32*8192,kb+a0*32*16384,l*16,w); dma_qkv(LR+SL,qb+(a0+1)*32*8192,kb+(a0+1)*32*16384,l*16,w);
  u32 po=O_PART+(4*g*32+h)*1024+w*256+c*16+a0*32*32768;
  u32x4 Of[4][2],Qf[4],Kf[8],Vf[4],pa,pb; f32x4 u[2][8],s0,s1,o[4],lsum; float mx;
#define MF "v_mfma_f32_16x16x32_bf16 "
  auto G=[&](int t,int n){ const int kk=n>>3,q=n&7;
    if(kk) asm volatile(MF "%0, %1, %2, %0":"+v"(u[t][q]):"v"(Of[kk][t]),"a"(Wf[q][kk])); else asm volatile(MF "%0, %1, %2, 0":"=&v"(u[t][q]):"v"(Of[kk][t]),"a"(Wf[q][kk])); };
  auto mz=[&](f32x4& d,u32x4 a,u32x4 b){ asm volatile(MF "%0, %1, %2, 0":"=&v"(d):"v"(a),"v"(b)); };
  auto mv=[&](f32x4& d,u32x4 a,u32x4 b){ asm volatile(MF "%0, %1, %2, %0":"+v"(d):"v"(a),"v"(b)); };
  auto tieo=[&](){ asm volatile("s_nop 7":"+v"(o[0]),"+v"(o[1]),"+v"(o[2]),"+v"(o[3]),"+v"(lsum)); };
  auto tieu=[&](int t){ asm volatile("":"+v"(u[t][0]),"+v"(u[t][1]),"+v"(u[t][2]),"+v"(u[t][3]),"+v"(u[t][4]),"+v"(u[t][5]),"+v"(u[t][6]),"+v"(u[t][7])); };
  auto PK=[&](int t,int i){ return u32x4{pk(u[t][0][i],u[t][1][i]),pk(u[t][2][i],u[t][3][i]),pk(u[t][4][i],u[t][5][i]),pk(u[t][6][i],u[t][7][i])}; };
  constexpr u32 LC=LR+2*SL; u32 pp_=0; u32x4 cv;
  if(tid<4) lw4(L,LC+tid*4,0);
  auto post=[&](u32 v){ lw4(L,LC+w*4,v); cbar(); cv=lr16(L,LC); cbar(); };
  auto publ=[&](u32 v){ const u32 m=min(min(cv.x,cv.y),min(cv.z,cv.w)); cbar(); st16(W,(m>pp_&&l<8)?pfo+l*16:0x80000000u,u32x4{e+m,e+m,e+m,e+m}); cbar(); pp_=v; };
  auto R=[&](u32 sb,int n){ const int f=n/3,k=n%3; if(n>=12) Vf[n-12]=lr16(L,sb+16384+(4*eh+n-12)*1024+l*16); else if(k==0) Qf[f]=lr16(L,sb+(4*qt+f)*1024+l*16);
    else if(k==1) Kf[4+f]=lr16(L,sb+8192+(4*qt+f)*1024+l*16); else Kf[f]=lr16(L,sb+8192+f*1024+l*16); };
  auto SM=[&](){ mz(s1,Kf[4],Qf[0]); mz(s0,Kf[0],Qf[0]); for(int f=1;f<4;f++){ mv(s1,Kf[4+f],Qf[f]); mv(s0,Kf[f],Qf[f]); } };
  auto X=[&](int n){
    if(n<4){ s1[n]=(4*g+n<=c)?s1[n]*sc:-1.0e9f; s0[n]=qt?s0[n]*sc:-1.0e9f; }
    else if(n==4) mx=fmaxf(fmaxf(fmaxf(s0[0],s0[1]),fmaxf(s0[2],s0[3])),fmaxf(fmaxf(s1[0],s1[1]),fmaxf(s1[2],s1[3])));
    else if(n==5) mx=x16(mx,true);
    else if(n==6) mx=x32(mx,true);
    else if(n<11) s0[n-7]=__builtin_amdgcn_exp2f(s0[n-7]-mx);
    else if(n<15) s1[n-11]=__builtin_amdgcn_exp2f(s1[n-11]-mx);
    else if(n==15) pa=pk8(s0,s1);
    else if(n==16){ const u32x4 q=pa; pa.x=qt?q.x:q.z; pa.y=qt?q.y:q.w; pa.z=qt?q.z:q.x; pa.w=qt?q.w:q.y; }
    else if(n==17){ pb=u32x4{pa.z,pa.w,pa.x,pa.y}; asm volatile("":"+v"(pb)); } };
  const u32 one=0x3f803f80u; u32x4 on={one,one,one,one};
  auto PVL=[&](){ asm volatile("s_nop 1":"+v"(pa),"+v"(pb),"+v"(on)); mz(lsum,on,pa); for(int k=0;k<2;k++){ mz(o[2*k],Vf[2*k],pa); mz(o[2*k+1],Vf[2*k+1],pb); } };
  vmw<6>(); bar();
  #pragma unroll 1
  for(int n=8*FS;n<512;n++) st16(W,po+((n>>3)*32+(n&3)+(n&4)*4)*32768,PZ);
  for(int n=0;n<16;n++) R(LR,n);
  SM(); asm volatile("s_nop 7":"+v"(s0),"+v"(s1));
  #pragma unroll
  for(int n=0;n<18;n++) X(n);
  PVL(); tieo();
  bool req=false; u32 sd=LR;

  #pragma unroll 1
  for(int a=a0;a<a1;a++){
    const u32 kl=LK+(a&1)*4096;
    if(req) vmw<9>();
    else{
      vmw<0>(); post(a); publ(a);
      if(a<FS) while(true){ const u32 fv=ld4(W,kfo); if(rdy(rfl(fv),e,a+1)||--tmo<0) break; __builtin_amdgcn_s_sleep(SLA); }

      while(true){ dmac(kl+w*1024,mb+a*131072,l*16); vmw<0>(); if(a<FS||!__any(umx(lr16(L,kl+w*1024+l*16),0)==~0u)||--tmo<0) break; }
    }
    bar();

    u32x4 T[2],km[2];
    for(int k=0;k<2;k++) T[k]=tr2(L,kl+k*2048+to,64);
    for(int k=0;k<2;k++) km[k]=lr16(L,kl+(2*eh+k)*1024+l*16);
    SB;
    const int nx=min(a+2,63); const u64 qs=uni(qb+nx*32*8192+w*2048),ks=uni(kb+nx*32*16384+w*4096),ms=uni(mb+(a+1)*131072); const u32 mq=rfl(sd+w*2048),mk=rfl(sd+8192+w*4096);
    auto DQ=[&](int i){ asm volatile("s_mov_b32 m0, %0\ns_nop 0\nglobal_load_lds_dwordx4 %1, %2 offset:%3 sc1"::"s"(i<2?mq:mk),"v"(l*16),"s"(i<2?qs:ks),"n"((i<2?i:i-2)*1024):"memory"); };
    sd^=LR^(LR+SL);
    SB;
    f32x4 sm,sn; mz(sm,T[0],Qf[0]); mz(sn,T[0],Qf[2]); mv(sm,T[1],Qf[1]); mv(sn,T[1],Qf[3]);
    asm volatile("s_nop 7":"+v"(sm),"+v"(sn));
    float m0=(sm[0]+sn[2])*sc,m1=(sm[1]+sn[3])*sc;
    const float mn=x32(x16(fmaxf(mx,fmaxf(m0,m1)),true),true);
    m0=__builtin_amdgcn_exp2f(m0-mn); m1=__builtin_amdgcn_exp2f(m1-mn);
    const float al=__builtin_amdgcn_exp2f(mx-mn);
    const float ls=__builtin_amdgcn_rcpf(al*lsum[0]+x32(x16(m0+m1,false),false)),as=al*ls;
    const u32 pm=pk(m0*ls,m1*ls);
    u32x4 bz={0,0,pm,0},bw={0,0,0,pm};
    for(int k=0;k<4;k++) o[k]*=as;
    asm volatile("s_nop 1":"+v"(bz),"+v"(bw),"+v"(o[0]),"+v"(o[1]),"+v"(o[2]),"+v"(o[3]));
    for(int k=0;k<2;k++){ mv(o[2*k],km[k],bz); mv(o[2*k+1],km[k],bw); }
    dmaf(LC+256+w*256,WSP,kfo);
    tieo();
    lw16(L,LO+(4*eh+qt)*1024+l*16,pk8(o[0],o[1]));
    lw16(L,LO+(4*eh+2+qt)*1024+l*16,pk8(o[2],o[3]));

    bar();
    for(int kk=0;kk<4;kk++) Of[kk][0]=lr16(L,LO+kk*2048+l*16);
    SB; DQ(0); SB;

    #pragma unroll
    for(int n=0;n<32;n++){
      if(n<12) R(sd,n); else if(n<16) Of[n-12][1]=lr16(L,LO+(n-12)*2048+1024+l*16); else if(n<20) R(sd,n-4);
      if(n%5==4&&n<25) DQ(n/5+1);
      SB; G(0,n); SB; }

    vmw<6>(); post(a); const u32 fv=lr4(L,LC+256+w*256); SB;
    SM(); SB;
    const u32 dq=rfl(fv)-e-(a+2),dm=a+1<a1?63-a:0; req=dq<dm;
    asm volatile("s_mov_b32 m0, %0\ns_cmp_lt_u32 %3, %4\ns_cselect_b64 exec, -1, 0\nglobal_load_lds_dwordx4 %1, %2 sc1\ns_mov_b64 exec, -1"::"s"(rfl((kl^4096)+w*1024)),"v"(l*16),"s"(ms),"s"(rfl(dq)),"s"(rfl(dm)):"memory","scc");
    SB;
    u32x4 d;
    #pragma unroll
    for(int n=0;n<32;n++){
      G(1,n); SB;
      const int i=n>>3,r=n&7;
      if(n==0) tieu(0);
      if(n==2) asm volatile("":"+v"(s0),"+v"(s1));
      if(r==0) d=PK(0,i); else if(r==1) st16(W,po+i*32768,d); else if(n==2) publ(a); else{ const int x=6*i+r-3; if(x<18) X(x); }
      SB; }

    PVL(); tieu(1); SB;
    for(int i=0;i<4;i++){ st16(W,po+(16+i)*32768,PK(1,i)); SB; }
    po+=32*32768; tieo();

  }
  vmw<0>(); post(a1); publ(a1);

}
DI float gelu(float v){ float u=v*(1.5957691216f+0.0713548163f*v*v); return v*__builtin_amdgcn_rcpf(1.f+__expf(-u)); }
template<bool z2> DI void win_role(const Args& A,rsrc W,ldsp L,u32 e,int z,int& tmo){
  const int tid=threadIdx.x,w=rfl(tid>>6),l=tid&63,g=l>>4,c=l&15,qq=c>>2,p=c&3;
  const int zi=z2?z-11:z, ft=4*zi+w;
  #pragma unroll 2
  for(int n=0;n<16;n++){ int i=16*w+n,rr=l>>3; u32 ch=(l&7)^((rr&2)|((n&1)<<2)); if(z==21&&ch>5) ch=5;
    dma(i*1024,A.win+(size_t)i*8*2752+(z2?1376:0)+zi*128,rr*2752+ch*16); }
  vmw<0>(); bar();
  u32x4 Wf[2][16];
  for(int a=0;a<2;a++){ u32 o=(8*g+qq)*128+(((2*(w^a)+(p>>1))^((qq&2)|((g&1)<<2)))<<4)+(p&1)*8; for(int ks=0;ks<16;ks++) Wf[a][ks]=tr2(L,o+ks*4096,512); }
  const int rho=8*w+(l>>3),tok=z2?8*((rho&15)>>2)+4*(rho>>4)+(rho&3):rho; const u32 vo=tok*1024+(((l&7)^((rho>>1)&7))<<4);
  const u32 ro0=c*128+((g^(c>>1))<<4)+(w&1)*2048, ro1=c*128+(((4+g)^(c>>1))<<4)+(w&1)*2048;
  constexpr u32 LS=0x10000,LD=0x11000,LE=0x12000;
  auto rfo=[&](int s){ return F_RF+((s&3)*32+tok)*128; };
  auto rows=[&](int s,int n0=0,int n1=8){ const u32 m=rfl((s&1)*32768+w*1024); const u64 b=uni(WSP+O_H2+s*32768);
    #pragma unroll
    for(int n=n0;n<n1;n++) asm volatile("s_add_u32 m0, %0, %3\ns_nop 0\nglobal_load_lds_dwordx4 %2, %1 offset:%4 sc1"::"s"(m),"s"(b),"v"(vo),"n"(n*3968),"n"(n*128):"scc","memory"); };
  if(tid==0) lw4(L,LD,0);
  #pragma unroll 1
  for(int s=FZ;s<64;s++){ if(ft<43) st16(W,(z2?O_Z2:O_Z1)+(s*43+ft)*1024+l*16,PZ); if(z2&&w==0) st16(W,O_ST+g*STC+(s*12+zi)*256+c*16,PZ); }
  bar();

  auto pub=[&](){ u32 cn=0; if(l==0) cn=__hip_atomic_fetch_add((__attribute__((address_space(3))) u32*)(L+LD),1u,__ATOMIC_RELAXED,__HIP_MEMORY_SCOPE_WORKGROUP);
    cn=rfl(cn); if((cn&3)==3) flag(W,F_ZF+z*128,e+(cn>>2)+1,l); };
  bool pend=false;
  waitf(W,rfo(0),true,e,1,tmo);
  rows(0); vmw<0>();
  #pragma unroll 1
  for(int s=0;s<64;s++){
    const u32 sb=(s&1)*32768;
    bar();
    u32x4 hf[16];
    for(int ks=0;ks<16;ks++) hf[ks]=lr16(L,sb+(ks>>1)*4096+((ks&1)?ro1:ro0));

    SB;
    f32x4 a0={0,0,0,0},a1=a0,b0=a0,b1=a0;
    #pragma unroll
    for(int ks=0;ks<16;ks+=2){
      if(z2){ a1=mm(hf[ks],Wf[1][ks],a1); b1=mm(hf[ks+1],Wf[1][ks+1],b1); }
      else  { a1=mm(Wf[1][ks],hf[ks],a1); b1=mm(Wf[1][ks+1],hf[ks+1],b1); }
      SB; rows(s+1,ks>>1,(ks>>1)+1); SB;
    }
    SB;
    #pragma unroll
    for(int ks=0;ks<16;ks+=2){
      if(z2){ a0=mm(hf[ks],Wf[0][ks],a0); b0=mm(hf[ks+1],Wf[0][ks+1],b0); }
      else  { a0=mm(Wf[0][ks],hf[ks],a0); b0=mm(Wf[0][ks+1],hf[ks+1],b0); }
    }
    for(int i=0;i<4;i++) a1[i]=gelu(a1[i]+b1[i]);
    lw8(L,LE+w*512+l*8,u32x2{pk(a1[0],a1[1]),pk(a1[2],a1[3])});
    for(int i=0;i<16;i++){ __builtin_amdgcn_sched_group_barrier(8,1,0); __builtin_amdgcn_sched_group_barrier(2,4,0); }
    SB;
    if(pend){ vmw<8>(); pub(); }
    bar();
    u32x2 zo=*(__attribute__((address_space(3))) u32x2*)(L+LE+(w^1)*512+l*8);
    SB;
    for(int i=0;i<4;i++) a0[i]=gelu(a0[i]+b0[i]);
    const u32 za=pk(a0[0],a0[1]),zb=pk(a0[2],a0[3]);
    u32x4 zv=(w&1)?u32x4{zo.x,zo.y,za,zb}:u32x4{za,zb,zo.x,zo.y};
    if(ft<43) st16(W,(z2?O_Z2:O_Z1)+(s*43+ft)*1024+l*16,zv); else zv=u32x4{0,0,0,0};

    if(z2){
      float f[8],q[8]; up8(zv,f);
      for(int i=0;i<8;i++) q[i]=f[i]*f[i];
      for(int i=0;i<8;i++){ float k=(c&8)?q[i]:f[i],o=(c&8)?f[i]:q[i]; f[i]=k+dpp<0x128>(o); }
      for(int i=0;i<4;i++){ float k=(c&4)?f[4+i]:f[i],o=(c&4)?f[i]:f[4+i]; f[i]=k+dpp<0x141>(dpp<0x1B>(o)); }
      for(int i=0;i<2;i++){ float k=(c&2)?f[2+i]:f[i],o=(c&2)?f[i]:f[2+i]; f[i]=k+dpp<0x4E>(o); }
      { float k=(c&1)?f[1]:f[0],o=(c&1)?f[0]:f[1]; f[0]=k+dpp<0xB1>(o); }
      lw4(L,LS+w*256+(8*g+(c&7))*8+(c>>3)*4,__builtin_bit_cast(u32,f[0]));
      bar();
      if(w==0){ f32x4 t={0,0,0,0}; for(int k=0;k<4;k++) t+=__builtin_bit_cast(f32x4,lr16(L,LS+k*256+c*16));
        st16(W,O_ST+g*STC+(s*12+zi)*256+c*16,__builtin_bit_cast(u32x4,t)); }
    }

    if(z2&&w==0) vmw<2>(); else if(ft<43) vmw<1>(); else vmw<0>();
    pend=true;
    while(true){
      u32 m=(s<3||s==63)?~0u:0; for(int n=0;n<8;n++) m=umx(lr16(L,(sb^32768)+n*4096+w*1024+l*16),m);
      if(!__any(m==~0u)) break;
      if(pend&&(s+1<FS||s<FZ||s==63)){ vmw<0>(); pub(); pend=false; }
      if(s==63||--tmo<0) break;
      if(s+1<FS) waitf(W,rfo(s+1),true,e,s+2,tmo);

      rows(s+1); vmw<0>(); if(s+1<FS) break; }

  }

}
DI void spat_role(const Args& A,rsrc W,ldsp L,u32 e,int j,int& tmo){
  const int tid=threadIdx.x,w=rfl(tid>>6),l=tid&63,g=l>>4,c=l&15,qq=c>>2,p=c&3,rb=j>>1,nh=j&1;
  constexpr u32 LW=45056;
  const char* wo=A.wout+nh*512;
  auto stage=[&](int k0,int nk,u32 dst){
    #pragma unroll 2
    for(int n=0;n<4*nk;n++){ int i=4*n+w,ks=k0+(n>>2)+4+11*nh; ks-=ks>21?22:0; int row=32*ks+(2*i&31)+(l>>5); u32 ch=(l&31)^((row&3)<<2); row=min(row,687);
      dma(dst+i*1024,wo,row*1024+ch*16); } };
  u32 wa[4]; for(int nt=0;nt<4;nt++) wa[nt]=(4*g+qq)*512+(((8*w+4*(nt>>1)+p)^(qq<<2))<<4)+(nt&1)*8;
  u32x4 Wf[17][4];
  stage(0,7,LW); vmw<0>(); bar();
  for(int k=0;k<7;k++) for(int nt=0;nt<4;nt++) Wf[k][nt]=tr2(L,LW+k*16384+wa[nt],8192);
  bar();
  constexpr u32 LS=LW,LT=0x5000,LX=0x9000,LF=0x9400;
  const u32 wvo=rb*131072+w*128+c*4096+g*16;
  auto ldw=[&](int q){ u32 lb=rfl(LS+(q&1)*16384+w*4096); asm volatile("":"+s"(lb)); for(int i=0;i<4;i++) dma(lb+i*1024,A.wsp,wvo+(i&1)*65536+q*512+(i>>1)*64); };
  int r=j&3;
  if(w==0){ dma(LX,A.x+(size_t)(r*32+(j>>2))*1024,l*16);
    #pragma unroll 1
    for(int n=r+4;n<64;n+=4) st16(W,O_H2+(n*32+(j>>2))*1024+l*16,PZ); }
  stage(7,7,LW); stage(21,1,0); vmw<0>(); bar();
  for(int k=0;k<7;k++) for(int nt=0;nt<4;nt++) Wf[7+k][nt]=tr2(L,LW+k*16384+wa[nt],8192);
  for(int nt=0;nt<4;nt++) Wf[16][nt]=tr2(L,wa[nt],8192);
  bar();
  stage(14,7,LW);
  { float t8[8]; ldmod(W,1024+512*w+8*l,e,t8,tmo); if(w==2) for(int i=0;i<8;i++) t8[i]+=1.f;
    lw16(L,w*2048+l*16,__builtin_bit_cast(u32x4,f32x4{t8[0],t8[1],t8[2],t8[3]})); lw16(L,w*2048+1024+l*16,__builtin_bit_cast(u32x4,f32x4{t8[4],t8[5],t8[6],t8[7]})); }
  vmw<0>(); bar();
  for(int k=0;k<2;k++) for(int nt=0;nt<4;nt++) Wf[14+k][nt]=tr2(L,LW+k*16384+wa[nt],8192);
  if(HN_&&w<2) waitf(W,F_PH+tid*128,true,e,HN_*(tid>>5)+HN_,tmo);
  bar(); ldw(0); vmw<0>();
  f32x4 acc[6][2],am[2];
  for(int k=0;k<6;k++) for(int t=0;t<2;t++) acc[k][t]=f32x4{0,0,0,0};
  am[0]=am[1]=f32x4{0,0,0,0};
  const u32 f0=22*nh+6*w;
  u32 zq[4]; for(int k=0;k<4;k++){ zq[k]=(k?min(f0+k+2,42u):f0)*1024+l*16; asm volatile("":"+v"(zq[k])); }
  auto ldq=[&](int k,u32 so){ return __builtin_amdgcn_raw_buffer_load_b128(W,k<3?zq[0]+k*1024:zq[k-2],so,16); };
  u32x4 z1[6]; float bs[2];
  constexpr u32 LR=0x2000,LD=0x4800; const u32 ro=g*512+(g&1)*16+(g>>1)*128;
  if(w==0) lw4(L,LF+l*4,e-1);
  u32 so=O_ST+(j&3)*STC+(3-w)*3072+(l&31)*8+(l>>5)*1536,pq=O_PART+((j>>2)*32+8*w)*1024+l*16,rq=F_RF+((j&3)*32+(j>>2))*128,xq=F_RF+((rb&3)*32+(l&31))*128; asm volatile("":"+v"(xq),"+v"(so),"+v"(pq),"+v"(rq));
  u32 ds=0,lt=LT; const u32 pofs=F_AG+(j&(8*NC_-1))*256+(l>>5)*128;

  auto wait=[&](int s){
    auto chk=[&](){ const u32 fw=lr4(L,LF+l*4); const u64 m=__ballot(rdy(fw,e,(l<32?r:s)+1)),m2=__ballot(rdy(fw,e,l<32?r-GT:s+2));
      if(r>=FS&&r<64&&(u32)m2==~0u) return 0u;
      return ((u32)m==~0u||r<4*HN_)?0u:(((u32)(m>>32)==~0u&&(s+1<r||r>63))?((PR&&(s&7)!=7&&s<62&&(u32)(m2>>32)==~0u&&(s+2<r||r>63))?3u:1u):((s>=FZ&&(r>63||s+GZ<r))?1u:2u)); };
    while(true){
      if(w==0){ u32 d=chk();
        while(d==2){ dmaf(LF,WSP,pofs); vmw<0>(); d=chk(); if(d==2) __builtin_amdgcn_s_sleep(SLP); if(--tmo<0) d=1; }
        if(l==0) lw4(L,LD+ds,d);
      }
      bar();
      const u32 d=rfl(lr4(L,LD+ds)); ds^=4;
      if(w==0) dmaf(LF,WSP,pofs);

      if(d) return d;

      const u32 row=r*32+(j>>2);
      u32x4 pv[8];
      while(true){ cbar(); for(int k=0;k<8;k++) pv[k]=ld16(W,pq+r*MB+k*1024);
        if(r<FS) break;
        u32 m=0; for(int k=0;k<8;k++) m=umx(pv[k],m);
        if(!__any(m==~0u)||--tmo<0) break; }
      float u[8]; up8(pv[0],u);
      for(int k=1;k<8;k++){ float f[8]; up8(pv[k],f); for(int i=0;i<8;i++) u[i]+=f[i]; }
      for(int i=0;i<8;i++) lw4(L,LR+w*2304+ro+(32*(i>>1)+4*(i&1)+8*qq+p)*4,__builtin_bit_cast(u32,u[i]));
      bar();

      if(w==0){
        f32x4 md[3][2],y[2]={{0,0,0,0},{0,0,0,0}};
        for(int k=0;k<4;k++) for(int q=0;q<2;q++) y[q]+=__builtin_bit_cast(f32x4,lr16(L,LR+k*2304+ro+c*32+q*16));
        for(int k=0;k<3;k++) for(int q=0;q<2;q++) md[k][q]=__builtin_bit_cast(f32x4,lr16(L,k*2048+q*1024+l*16));
        const u32x4 xv=lr16(L,LX+l*16);
        y[0]=f32x4{lo(xv.x),hi(xv.x),lo(xv.y),hi(xv.y)}+md[0][0]*y[0]; y[1]=f32x4{lo(xv.z),hi(xv.z),lo(xv.w),hi(xv.w)}+md[0][1]*y[1];
        f32x4 t=y[0]+y[1]; const float m=wsum(t.x+t.y+t.z+t.w)*(1.f/512);
        f32x4 d0=y[0]-m,d1=y[1]-m; t=d0*d0+d1*d1; const float rs=rsqrtf(wsum(t.x+t.y+t.z+t.w)*(1.f/512)+1e-5f);
        st16(W,O_H2+row*1024+l*16,pk8(d0*rs*md[2][0]+md[1][0],d1*rs*md[2][1]+md[1][1]));
        st16(W,O_X1+row*1024+l*16,pk8(y[0],y[1]));

        vmw<0>();
        flag(W,rq,e+r+1,l);
        dma(LX,A.x,min(row+128,2047u)*1024+l*16);

      }
      r+=4;
    }
  };
  auto seg=[&](int s,bool two){ const bool fast=s>=FZ,last=s==63;
    if(fast) two=s<62&&(s&7)!=7;
    lt^=2048; u32 m2=0;
    u32x4 wv[2]; for(int t=0;t<2;t++) wv[t]=lr16(L,LS+(s&15)*2048+t*1024+l*16);
    u32x2 sv[6]; u32x4 zf[6];
    const bool sw=w==3||(w==2&&two);
    if(last&&w==1) waitf(W,xq,true,e,rb+1,tmo);
    while(true){ cbar();
      if(sw) for(int k=0;k<6;k++) sv[k]=ld8n(W,so+s*3072+(k<5?k*256:(l<32?1280:1024)));
      SB;
      for(int k=0;k<6;k++) zf[k]=ldq(k,O_Z2+s*44032);
      SB;
      if(two||last) for(int k=0;k<6;k++) z1[k]=ldq(k,last?O_Z1+rb*44032:O_Z2+(s+1)*44032);
      else for(int k=0;k<6;k++) asm volatile("":"=v"(z1[k]));
      SB;
      if(!fast) break;
      u32 m=0; if(sw) for(int k=0;k<6;k+=2) m=umx(u32x4{sv[k].x,sv[k].y,sv[k+1].x,sv[k+1].y},m);
      m2=w==2?m:0; m=w==2?0:m;
      for(int k=0;k<6;k++) m=umx(zf[k],m);
      for(int k=0;k<6;k++) m2=umx(z1[k],m2);
      if(last) m=max(m,m2);
      if(!__any(m==~0u)||--tmo<0) break; __builtin_amdgcn_s_sleep(SLZ); }
    if(sw){ f32x2 st=__builtin_bit_cast(f32x2,sv[0]);
      for(int k=1;k<5;k++) st+=__builtin_bit_cast(f32x2,sv[k]);
      if(l<32) st+=__builtin_bit_cast(f32x2,sv[5]);
      const float mu=x32(st.x,false)*(1.f/688), rs=rsqrtf(fmaxf(x32(st.y,false)*(1.f/688)-mu*mu,0.f)+1e-5f);
      const u32 hl=pk(mu,mu-(float)(__bf16)mu);
      lw4(L,lt+w*512+l*4,__builtin_bit_cast(u32,rs)); lw2(L,lt+w*512+256+l*2,l<32?hl:hl>>16); }
    lw4(L,lt+w*4,__any(m2==~0u));
    bar();
    two=two&&rfl(umx(lr16(L,lt),0))==0;

    #pragma unroll
    for(int n=0;n<2;n++) if(n==0||two){
    if(n) for(int t=0;t<2;t++) wv[t]=lr16(L,LS+((s+1)&15)*2048+t*1024+l*16);
    const f32x4 ra=__builtin_bit_cast(f32x4,lr16(L,lt+(3-n)*512+g*32)),rc=__builtin_bit_cast(f32x4,lr16(L,lt+(3-n)*512+g*32+16));
    const u32x4 mf=lr16(L,lt+(3-n)*512+256+(c&1)*64+g*16);
    const float r8[8]={ra.x,ra.y,ra.z,ra.w,rc.x,rc.y,rc.z,rc.w};
    for(int t=0;t<2;t++){ float f[8]; up8(wv[t],f); u32x4 b; for(int i=0;i<4;i++) b[i]=pk(f[2*i]*r8[2*i],f[2*i+1]*r8[2*i+1]);
      am[t]=mm(mf,b,am[t]);
      for(int k=0;k<6;k++) acc[k][t]=mm(n?z1[k]:zf[k],b,acc[k][t]); }
    SB;

    }
    if((s&7)==0&&s<56) ldw((s>>3)+1);
    return two;
  };
  for(int t=0;t<2;t++) bs[t]=lo(*(const u16*)(A.bsp+(32*rb+16*t+c)*2));
  #pragma unroll 1
  for(int s=0;s<64;s++){ const bool two=(s<=FZ||r<64)&&wait(s)==3; s+=seg(s,two); }

  bar();
  for(int t=0;t<2;t++){
    const float d=bs[t]-am[t][0]-am[t][1];
    for(int q=0;q<3;q++){ u32x4 u;
      for(int o=0;o<2;o++){ const int k=2*q+o; u32 za=z1[k][2*t],zb=z1[k][2*t+1]; f32x4 v=acc[k][t];
        u[2*o]=pk(lo(za)*(v.x+d),hi(za)*(v.y+d)); u[2*o+1]=pk(lo(zb)*(v.z+d),hi(zb)*(v.w+d)); }
      if(q==1&&f0==40) u.z=u.w=0;
      st16(W,O_U+j*24576+((3*w+q)*2+t)*1024+l*16,u);
      if(q<2||w<3) lw16(L,((3*w+q)*2+t)*1024+l*16,u); }
  }

  cbar();
  u32x4 xf[2][2],gv[2][2];
  for(int t=0;t<2;t++) for(int np=0;np<2;np++) xf[t][np]=ld16(W,O_X1+(32*rb+16*t+c)*1024+(256*nh+64*w+32*np+8*g)*2);
  for(int np=0;np<2;np++) for(int a=0;a<2;a++) gv[np][a]=ld16(W,F_MOD+(2560+256*nh+64*w+32*np+8*g+4*a)*4);
  bar();
  f32x4 oc[4][2]; for(int nt=0;nt<4;nt++) for(int t=0;t<2;t++) oc[nt][t]=f32x4{0,0,0,0};
  auto gem=[&](int k0,int k1){
    u32x4 u[12][2]; for(int t=0;t<2;t++) u[0][t]=lr16(L,k0*2048+t*1024+l*16);
    #pragma unroll
    for(int kk=k0;kk<k1;kk++){
      if(kk+1<k1) for(int t=0;t<2;t++) u[kk+1-k0][t]=lr16(L,(kk+1)*2048+t*1024+l*16);
      #pragma unroll
      for(int nt=0;nt<4;nt++){ u32x4 wf; if(kk>2&&kk<20) wf=Wf[kk>3?kk-4:16][nt]; else wf=tr2(L,LW+(kk>2?kk-18:kk+4)*16384+wa[nt],8192);
        oc[nt][0]=mm(wf,u[kk-k0][0],oc[nt][0]); oc[nt][1]=mm(wf,u[kk-k0][1],oc[nt][1]); }
    } };
  gem(3,7); SB;
  vmw<8>(); flag(W,F_UF+(j*4+w)*128,e,l);
  SB; gem(0,3); SB;

  while(true){ if(rfl(ld4(W,F_UF+((j^1)*4+w)*128))==e||--tmo<0) break; }
  for(int q=0;q<6;q++) if(q<4||w<3) dmac((22+6*w+q)*1024,WSP+O_U+(j^1)*24576+(6*w+q)*1024,l*16);
  SB; gem(7,11); SB;
  vmw<0>(); bar();

  gem(11,22);

  for(int t=0;t<2;t++) for(int np=0;np<2;np++){ f32x4 r[2];
    for(int a=0;a<2;a++){ f32x4 x={lo(xf[t][np][2*a]),hi(xf[t][np][2*a]),lo(xf[t][np][2*a+1]),hi(xf[t][np][2*a+1])}; f32x4 gb={lo(gv[np][a].x),hi(gv[np][a].x),lo(gv[np][a].z),hi(gv[np][a].z)}; r[a]=x+gb*oc[2*np+a][t]; }
    *(u32x4*)(A.out+(size_t)(32*rb+16*t+c)*1024+(256*nh+64*w+32*np+8*g)*2)=pk8(r[0],r[1]); }
  vmw<0>();

}
DI void agg_role(const Args& A,rsrc W,ldsp L,u32 e,int z,int& tmo){
  const int l=threadIdx.x; const u32 pofs=F_PF+(z?32+min(l,21):(l&31))*128;
  u32 cp=0,b=0;
  dmaf(0,WSP,pofs);
  while(cp<64&&--tmo>=0){
    __builtin_amdgcn_s_sleep(AD);
    b^=256; dmaf(b,WSP,pofs); vmw<1>();
    const u32 v=lr4(L,(b^256)+l*4),c0=cp;
    while(__all(rdy(v,e,cp+1))){ cp++; }
    if(cp!=c0){ cbar(); for(int i=0;i<NC_;i++) st16(W,F_AG+z*128+(i*8+(l>>3))*256+(l&7)*16,u32x4{e+cp,e+cp,e+cp,e+cp});
    }
  }
}

__global__ __launch_bounds__(256,1) void mega(Args A){
  __shared__ __attribute__((aligned(16))) char lds[163840];
  ldsp L=(ldsp)lds;
  const rsrc W=__builtin_amdgcn_make_buffer_rsrc(WSP,0,WS_USED,0x00020000);
  const int b=blockIdx.x,r=b&7,i=b>>3;

  const u32 e=(rfl(ld4(W,F_EP))&~127u)+128u;
  if((threadIdx.x&63)==0) st4(W,F_STA+b*16+(threadIdx.x>>4),e);
  int tmo=TMO,jb=r-2;
  if(b==0&&threadIdx.x==0) st4(W,F_ROT,e+(__builtin_amdgcn_s_getreg(20|(3<<11))&1));
  if(r==1||r==7){ u32 v; while(true){ v=rfl(ld4(W,F_ROT))-e; if(v<2||--tmo<0) break; __builtin_amdgcn_s_sleep(1); } jb=(r==1)==(v==0)?-1:5; }
  const int k=i*6+jb;
  if(jb<0){ chain_role(A,W,L,e,(jb+2)*32+i); return; }
  const int j=jb*32+i;
  if(j<64) ln1_role(A,W,L,e,j,tmo);
  const int sl=j>>1;
  if(sl<64) proj_loop<false>(A,W,L,e,sl>>5,sl&31,j&1,tmo); else proj_loop<true>(A,W,L,e,2,sl&31,j&1,tmo);
  if(k<32||(HN_&&k>=64)){ const int q=(k>>5)-2; attn_role(A,W,L,e,k&31,k<32?4*HN_:q*HN_,k<32?64:q*HN_+HN_,k<32?F_PF+k*128:F_PH+(k-64)*128,tmo); bar(); }
  if(k<32);
  else if(k<43) win_role<false>(A,W,L,e,k-32,tmo);
  else if(k<54) win_role<true>(A,W,L,e,k-32,tmo);
  else if(k>=64) spat_role(A,W,L,e,k-64,tmo);
  else if(k==55||k==56){ if(threadIdx.x<64) agg_role(A,W,L,e,k-55,tmo); }
  else if(k==54&&threadIdx.x<64){
    for(int q=0;q<4;q++) while(true){ cbar(); u32x4 v=ld16(W,F_STA+q*1024+threadIdx.x*16); cbar(); if(__all(v.x==e&&v.y==e&&v.z==e&&v.w==e)||--tmo<0) break; __builtin_amdgcn_s_sleep(8); }
    if(threadIdx.x==0) st4(W,F_EP,e);
  }

}
extern "C" void kernel_launch(void* const* d_in,const int* in_sizes,int n_in,void* d_out,int out_size,void* d_ws,size_t ws_size,hipStream_t stream){
  Args A;
  A.x=(const char*)d_in[0]; A.mem0=(const char*)d_in[1]; A.Wq=(const char*)d_in[2]; A.Wk=(const char*)d_in[3]; A.Wv=(const char*)d_in[4]; A.Wo=(const char*)d_in[5];
  A.win=(const char*)d_in[8]; A.wsp=(const char*)d_in[9]; A.bsp=(const char*)d_in[10]; A.wout=(const char*)d_in[11]; A.c=(const char*)d_in[12]; A.wmod=(const char*)d_in[13];
  A.out=(char*)d_out; A.ws0=(char*)d_ws;
  hipLaunchKernelGGL(mega,dim3(256),dim3(256),0,stream,A);
}
```
